```python
import torch

def _softmax(x, axis=-1):
    return torch.softmax(x, dim=axis)
def _gelu(x):
    return torch.nn.functional.gelu(x, approximate="tanh")
def _silu(x):
    return torch.nn.functional.silu(x)
def _sigmoid(x):
    return torch.sigmoid(x)
def _layernorm(x, axis=-1, eps=1e-5):
    m = x.mean(dim=axis, keepdim=True)
    v = x.var(dim=axis, keepdim=True, unbiased=False)
    return (x - m) / torch.sqrt(v + eps)
def _scatter_add(tgt, idx, src, axis=0):
    return tgt.clone().index_add_(axis, idx, src)
def _segment_sum(src, seg, n):
    out = torch.zeros((n,) + src.shape[1:], dtype=src.dtype)
    return out.index_add_(0, seg, src)
def _pad(x, pw):
    flat = tuple(v for pair in reversed(pw) for v in pair)
    return torch.nn.functional.pad(x, flat)

kernel_name = 'recurrent_scan__sliding_block_cached__glu_gated_swiglu__decode'

def setup_inputs():

    _w = (lambda a: (lambda t: t.to(torch.bfloat16) if t.is_floating_point() else t)(torch.as_tensor(a)))
    def _A_setup():
        B, Sq, D = 1, 1, 576
        Hq, Hkv, Sk, Dh = 32, 16, 65536, 128
        assert Hq % Hkv == 0, (Hq, Hkv)
        return {
            "x": _w((lambda *s: torch.randn(*s, dtype=torch.bfloat16) * 0.05)(B, Sq, D)),
            "k_cache": _w((lambda *s: torch.randn(*s, dtype=torch.bfloat16) * 3.2)(B, Hkv, Sk, Dh)),
            "v_cache": _w((lambda *s: torch.randn(*s, dtype=torch.bfloat16) * 3.2)(B, Hkv, Sk, Dh)),
            "wq": _w((lambda *s: torch.randn(*s, dtype=torch.bfloat16) * 3.2)(D, Hq * Dh) * (D ** -0.5)),
            "wo": _w((lambda *s: torch.randn(*s, dtype=torch.bfloat16) * 3.2)(Hq * Dh, D) * ((Hq * Dh) ** -0.5)),
        }
    _A_in = _A_setup()

    def _B_setup():
        B, S, D, F = 1, 1, 576, 4608
        return {
            "x": _w((lambda *s: torch.randn(*s, dtype=torch.bfloat16) * 0.05)(B, S, D)),
            "w1": _w((lambda *s: torch.randn(*s, dtype=torch.bfloat16) * 0.4)(D, F) * (2.0 / D) ** 0.5),
            "w3": _w((lambda *s: torch.randn(*s, dtype=torch.bfloat16) * 0.4)(D, F) * (2.0 / D) ** 0.5),
            "w2": _w((lambda *s: torch.randn(*s, dtype=torch.bfloat16) * 0.4)(F, D) * (2.0 / F) ** 0.5),
        }
    _B_in = _B_setup()

    return {
        "A_x": _A_in["x"],
        "A_k_cache": _A_in["k_cache"],
        "A_v_cache": _A_in["v_cache"],
        "A_wq": _A_in["wq"],
        "A_wo": _A_in["wo"],
        "B_x": _B_in["x"],
        "B_w1": _B_in["w1"],
        "B_w3": _B_in["w3"],
        "B_w2": _B_in["w2"],
    }

def reference(A_x, A_k_cache, A_v_cache, A_wq, A_wo, B_x, B_w1, B_w3, B_w2):
    def _A_ref(x, k_cache, v_cache, wq, wo):
        B, Sq, D = x.shape
        Hq, Dh = 32, 128
        Hkv, Sk = k_cache.shape[1], k_cache.shape[2]
        W = min(4096, max(256, Sk // 4))
        rep = Hq // Hkv
        q = (lambda x, *d: x.permute(*d))((x @ wq).reshape(B, Sq, Hq, Dh), 0, 2, 1, 3)
        k = (lambda x, n, axis: torch.repeat_interleave(x, n, dim=axis))(k_cache[:, :, Sk - W:, :], rep, axis=1)
        v = (lambda x, n, axis: torch.repeat_interleave(x, n, dim=axis))(v_cache[:, :, Sk - W:, :], rep, axis=1)
        scores = torch.einsum("bhqd,bhkd->bhqk", q, k) * (Dh ** -0.5)
        p = _softmax(scores, axis=-1)
        o = torch.einsum("bhqk,bhkd->bhqd", p, v)
        return (lambda x, *d: x.permute(*d))(o, 0, 2, 1, 3).reshape(B, Sq, Hq * Dh) @ wo

    def _B_ref(x, w1, w3, w2):
        gate = torch.einsum("bsd,df->bsf", x, w1)
        up = torch.einsum("bsd,df->bsf", x, w3)
        h = _silu(gate) * up
        out = torch.einsum("bsf,fd->bsd", h, w2)
        return out

    x = A_x
    xn = _layernorm(x, axis=-1)
    s = _A_ref(xn, A_k_cache, A_v_cache, A_wq, A_wo)
    g = _silu(_B_ref(xn, B_w1, B_w3, B_w2))
    return x + s * g
```

```cpp
#include <hip/hip_runtime.h>
#include <hip/hip_bf16.h>
#include <stdint.h>
#define EMU 0
typedef unsigned short u16;
constexpr int D = 576, HQ = 32, HKV = 16, SK = 65536, DH = 128, W = 4096, F = 4608;
__device__ __forceinline__ float bf2f(u16 b) { return __uint_as_float((unsigned)b << 16); }
__device__ __forceinline__ float rbf(float f) {
  return f;
}
__device__ __forceinline__ float wsum(float v) { for (int o = 32; o; o >>= 1) v += __shfl_xor(v, o); return v; }
__device__ __forceinline__ float wmax(float v) { for (int o = 32; o; o >>= 1) v = fmaxf(v, __shfl_xor(v, o)); return v; }

__device__ void ln(const u16* x, float* xs) {
  int lane = threadIdx.x & 63;
  float v[9]; float s = 0;
  for (int i = 0; i < 9; ++i) { v[i] = bf2f(x[lane + 64 * i]); s += v[i]; }
  s = wsum(s); float mf = s / D; float m = rbf(mf);
  float q = 0; for (int i = 0; i < 9; ++i) { float d = v[i] - mf; q += d * d; }
  q = wsum(q); float var = rbf(q / D);
  float sd = rbf(sqrtf(rbf(var + 1e-5f)));
  if (threadIdx.x < 64) for (int i = 0; i < 9; ++i) xs[lane + 64 * i] = rbf(rbf(v[i] - m) / sd);
  __syncthreads();
}
__global__ void k1(const u16* x, const u16* wq, const u16* w1, const u16* w3, float* q, float* h) {
  __shared__ float xs[D];
  ln(x, xs);
  int c = blockIdx.x * 64 + (threadIdx.x & 63); int w = threadIdx.x >> 6;
  __shared__ float red[2][4][64];
  float a = 0, b = 0;
  if (c < 4096) { for (int i = w * 144; i < w * 144 + 144; ++i) a += xs[i] * bf2f(wq[(size_t)i * 4096 + c]); }
  else { int f = c - 4096; for (int i = w * 144; i < w * 144 + 144; ++i) { a += xs[i] * bf2f(w1[(size_t)i * F + f]); b += xs[i] * bf2f(w3[(size_t)i * F + f]); } }
  red[0][w][threadIdx.x & 63] = a; red[1][w][threadIdx.x & 63] = b;
  __syncthreads();
  if (w == 0) {
    int l = threadIdx.x;
    a = red[0][0][l] + red[0][1][l] + red[0][2][l] + red[0][3][l];
    b = red[1][0][l] + red[1][1][l] + red[1][2][l] + red[1][3][l];
    if (c < 4096) q[c] = rbf(a);
    else { float g = rbf(a), u = rbf(b); float sg = rbf(g / (1.f + expf(-g))); h[c - 4096] = rbf(sg * u); }
  }
}
__global__ void k2(const float* q, const u16* kc, float* sc) {
  int g = blockIdx.y; int key = blockIdx.x * 256 + threadIdx.x;
  __shared__ float qs[256];
  qs[threadIdx.x] = q[g * 256 + threadIdx.x]; __syncthreads();
  const u16* kr = kc + ((size_t)g * SK + (SK - W) + key) * DH;
  float a = 0, b = 0;
  for (int d = 0; d < DH; ++d) { float k = bf2f(kr[d]); a += qs[d] * k; b += qs[128 + d] * k; }
  sc[(2 * g) * W + key] = rbf(rbf(a) * 0.08838834764831845f);
  sc[(2 * g + 1) * W + key] = rbf(rbf(b) * 0.08838834764831845f);
}
__global__ void k3(const float* sc, const u16* vc, float* o) {
  int h = blockIdx.x, g = h >> 1; int t = threadIdx.x;
  __shared__ float p[W]; __shared__ float red[16]; __shared__ float acc[8][128];
  float m = -INFINITY; for (int i = t; i < W; i += 1024) m = fmaxf(m, sc[h * W + i]);
  m = wmax(m); if ((t & 63) == 0) red[t >> 6] = m; __syncthreads();
  m = red[0]; for (int i = 1; i < 16; ++i) m = fmaxf(m, red[i]); __syncthreads();
  float s = 0; for (int i = t; i < W; i += 1024) { float e = expf(sc[h * W + i] - m); p[i] = e; s += e; }
  s = wsum(s); if ((t & 63) == 0) red[t >> 6] = s; __syncthreads();
  s = 0; for (int i = 0; i < 16; ++i) s += red[i];
  int d = t & 127, part = t >> 7; float a = 0;
  const u16* vr = vc + ((size_t)g * SK + (SK - W)) * DH;
  for (int j = part * 512; j < part * 512 + 512; ++j) a += rbf(p[j] / s) * bf2f(vr[(size_t)j * DH + d]);
  acc[part][d] = a; __syncthreads();
  if (t < 128) { float r = 0; for (int i = 0; i < 8; ++i) r += acc[i][t]; o[h * 128 + t] = rbf(r); }
}
__global__ void k4(const u16* x, const float* o, const float* h, const u16* wo, const u16* w2, __hip_bfloat16* out) {
  int c = blockIdx.x * 64 + (threadIdx.x & 63); int w = threadIdx.x >> 6;
  __shared__ float red[2][16][64];
  float a = 0, b = 0;
  for (int i = w * 256; i < w * 256 + 256; ++i) a += o[i] * bf2f(wo[(size_t)i * D + c]);
  for (int i = w * 288; i < w * 288 + 288; ++i) b += h[i] * bf2f(w2[(size_t)i * D + c]);
  red[0][w][threadIdx.x & 63] = a; red[1][w][threadIdx.x & 63] = b; __syncthreads();
  if (w == 0) {
    a = 0; b = 0; for (int i = 0; i < 16; ++i) { a += red[0][i][threadIdx.x]; b += red[1][i][threadIdx.x]; }
    float s = rbf(a), f = rbf(b); float g = rbf(f / (1.f + expf(-f)));
    out[c] = __float2bfloat16(bf2f(x[c]) + rbf(s * g));
  }
}
extern "C" void kernel_launch(void* const* d_in, const int* in_sizes, int n_in, void* d_out, int out_size, void* d_ws, size_t ws_size, hipStream_t stream) {
  const u16* x = (const u16*)d_in[0]; const u16* kc = (const u16*)d_in[1]; const u16* vc = (const u16*)d_in[2];
  const u16* wq = (const u16*)d_in[3]; const u16* wo = (const u16*)d_in[4];
  const u16* w1 = (const u16*)d_in[6]; const u16* w3 = (const u16*)d_in[7]; const u16* w2 = (const u16*)d_in[8];
  float* ws = (float*)d_ws; float* q = ws; float* h = ws + 4096; float* o = ws + 16384; float* sc = ws + 32768;
  k1<<<(4096 + F) / 64, 256, 0, stream>>>(x, wq, w1, w3, q, h);
  k2<<<dim3(16, 16), 256, 0, stream>>>(q, kc, sc);
  k3<<<32, 1024, 0, stream>>>(sc, vc, o);
  k4<<<9, 1024, 0, stream>>>(x, o, h, wo, w2, (__hip_bfloat16*)d_out);
}
```

```cpp
#include <hip/hip_runtime.h>
#include <stdint.h>

typedef unsigned u32;
typedef unsigned long long u64;
typedef u32 v4u __attribute__((ext_vector_type(4)));
typedef u32 v2u __attribute__((ext_vector_type(2)));
typedef float f4 __attribute__((ext_vector_type(4)));
typedef float f2 __attribute__((ext_vector_type(2)));
typedef __bf16 bf8 __attribute__((ext_vector_type(8)));
typedef __bf16 bf2 __attribute__((ext_vector_type(2)));
#define LAS __attribute__((address_space(3)))
#define GAS __attribute__((address_space(1)))
#define CAS __attribute__((address_space(4)))
typedef __amdgpu_buffer_rsrc_t rsrc_t;

#define STAMPS 0

constexpr int DM = 576, SK = 65536, WIN = 4096, FF = 4608;
constexpr int AUX_NT = 2, AUX_SC1 = 16;
constexpr int WS_EBOX = 4096;
constexpr int WS_ML = 65536;
constexpr int WS_OBOX = 131072;
constexpr int WS_FBOX = 131072 + 524288;
constexpr int WS_END = WS_FBOX + 36 * 36 * 128;
constexpr int WS_STAMP = 1 << 20;
constexpr int SPIN_CAP = 1 << 18;
constexpr int L_XN = 0;
constexpr int L_RED = 2560;
constexpr int L_ORED = 8704;
constexpr int L_M = 12800;
constexpr int L_CNT = 13056;
constexpr int LDS_BYTES = 96 * 1024;

__device__ __forceinline__ rsrc_t mk_rsrc(const void* p, u32 bytes) { return __builtin_amdgcn_make_buffer_rsrc((void*)p, 0, bytes, 0x00020000); }
__device__ __forceinline__ v4u ld16(rsrc_t r, int v, int s) { return __builtin_amdgcn_raw_buffer_load_b128(r, v, s, AUX_NT); }
__device__ __forceinline__ v4u ld16c(rsrc_t r, int v, int s) { return __builtin_amdgcn_raw_buffer_load_b128(r, v, s, AUX_SC1); }
__device__ __forceinline__ v2u ld8c(rsrc_t r, int v, int s) { return __builtin_amdgcn_raw_buffer_load_b64(r, v, s, AUX_SC1); }
__device__ __forceinline__ void st16c(v4u x, rsrc_t r, int v, int s) { __builtin_amdgcn_raw_buffer_store_b128(x, r, v + s, 0, AUX_SC1); }
__device__ __forceinline__ void st8c(v2u x, rsrc_t r, int v, int s) { __builtin_amdgcn_raw_buffer_store_b64(x, r, v, s, AUX_SC1); }
__device__ __forceinline__ float asf(u32 x) { return __builtin_bit_cast(float, x); }
__device__ __forceinline__ u32 asu(float x) { return __builtin_bit_cast(u32, x); }
__device__ __forceinline__ u32 pkbf(float lo, float hi) { bf2 r = {(__bf16)lo, (__bf16)hi}; return __builtin_bit_cast(u32, r); }
__device__ __forceinline__ float rbf(float x) { return asf(pkbf(x, x) << 16); }
template <int C> __device__ __forceinline__ u32 dppu(u32 x) { return (u32)__builtin_amdgcn_mov_dpp((int)x, C, 0xf, 0xf, true); }
template <int C> __device__ __forceinline__ float dppf(float x) { return asf(dppu<C>(asu(x))); }
constexpr int XOR1 = 0xB1, XOR2 = 0x4E, HMIR = 0x141, ROR8 = 0x128;
__device__ __forceinline__ float x16_sum(float x) { auto s = __builtin_amdgcn_permlane16_swap(asu(x), asu(x), false, false); return asf(s[0]) + asf(s[1]); }
__device__ __forceinline__ float x32_sum(float x) { auto s = __builtin_amdgcn_permlane32_swap(asu(x), asu(x), false, false); return asf(s[0]) + asf(s[1]); }
__device__ __forceinline__ float x16_max(float x) { auto s = __builtin_amdgcn_permlane16_swap(asu(x), asu(x), false, false); return fmaxf(asf(s[0]), asf(s[1])); }
__device__ __forceinline__ float x32_max(float x) { auto s = __builtin_amdgcn_permlane32_swap(asu(x), asu(x), false, false); return fmaxf(asf(s[0]), asf(s[1])); }
__device__ __forceinline__ float sum8(float x) { x += dppf<XOR1>(x); x += dppf<XOR2>(x); x += dppf<HMIR>(x); return x; }
__device__ __forceinline__ float max8(float x) { x = fmaxf(x, dppf<XOR1>(x)); x = fmaxf(x, dppf<XOR2>(x)); x = fmaxf(x, dppf<HMIR>(x)); return x; }
__device__ __forceinline__ float sum64(float x) { x = sum8(x); x += dppf<ROR8>(x); return x32_sum(x16_sum(x)); }
__device__ __forceinline__ float dot2(u32 a, u32 b, float c) { return __builtin_amdgcn_fdot2_f32_bf16(__builtin_bit_cast(bf2, a), __builtin_bit_cast(bf2, b), c, false); }
__device__ __forceinline__ f4 mfma(v4u a, v4u b, f4 c) { return __builtin_amdgcn_mfma_f32_16x16x32_bf16(__builtin_bit_cast(bf8, a), __builtin_bit_cast(bf8, b), c, 0, 0, 0); }
template <int T> __device__ __forceinline__ v4u onehot(u32 p, u32 pm, const v4u& mask) {
  constexpr int a = (T & 3) * 0x55, b = (3 - (T & 3)) * 0x55;
  v4u r;
  if (T < 4) { r.x = dppu<a>(p) & mask.x; r.y = dppu<a>(p) & mask.y; r.z = dppu<b>(pm) & mask.z; r.w = dppu<b>(pm) & mask.w; }
  else { r.x = dppu<b>(pm) & mask.x; r.y = dppu<b>(pm) & mask.y; r.z = dppu<a>(p) & mask.z; r.w = dppu<a>(p) & mask.w; }
  return r;
}
#define FENCE() asm volatile("" ::: "memory")
#define SB() __builtin_amdgcn_sched_barrier(0)
#define LGKM0() asm volatile("s_waitcnt lgkmcnt(0)" ::: "memory")
#define BAR() do { LGKM0(); __builtin_amdgcn_s_barrier(); FENCE(); } while (0)
#define BAR1() do { asm volatile("s_waitcnt lgkmcnt(0)\n\ts_barrier" ::: "memory"); } while (0)
#define MKTAG() ({ u32 t_; asm volatile("s_mul_i32 %0, %1, 0x19660d\n\ts_add_u32 %0, %0, 0x3c6ef35f" : "=s"(t_) : "s"(epoch) : "scc"); t_; })
constexpr int OOB = 0x70000000;
template <int I, int N, class F> __device__ __forceinline__ void sfor(F&& f) { if constexpr (I < N) { f(std::integral_constant<int, I>{}); sfor<I + 1, N>(f); } }

#define STAMP(i) do {} while (0)
#define STAMP_OUT() do {} while (0)

__global__ void __launch_bounds__(512, 2)
fused_decode(const char* x, const char* kc, const char* vc, const char* wq, const char* wo, const char* w1, const char* w3, const char* w2, char* out, char* ws) {
  extern __shared__ __attribute__((aligned(16))) char lds_[];
  LAS char* lds = (LAS char*)lds_;
  volatile LAS u32* cnt = (volatile LAS u32*)(lds + L_CNT);
  const int tid = threadIdx.x, lane = tid & 63, wave = __builtin_amdgcn_readfirstlane(tid >> 6), w = wave & 3;
  const int b = blockIdx.x, g = b & 15, s = b >> 4;
  STAMP(0);
  u32 epoch;
  asm volatile("s_load_dword %0, %10, 0x0" : "=&s"(epoch), "+s"(x), "+s"(kc), "+s"(vc), "+s"(wq), "+s"(wo), "+s"(w1), "+s"(w3), "+s"(w2), "+s"(out), "+s"(ws));
  const rsrc_t rw = mk_rsrc(ws, WS_END);
  const int r = lane >> 3, c8 = lane & 7;
  v4u mask; { const u32 hm = (lane & 1) ? 0xFFFF0000u : 0x0000FFFFu; const int q = c8 >> 1;
    mask.x = q == 0 ? hm : 0u; mask.y = q == 1 ? hm : 0u; mask.z = q == 2 ? hm : 0u; mask.w = q == 3 ? hm : 0u; }
  const bool dvalid = (lane >> 5) == (r & 1);
  const int dpos = ((lane >> 5) * 64 + c8 * 8 + ((lane >> 4) & 1) * 4) * 4;

  if (wave < 4) {
    u32 tag;
    if (s < 13) {
      const char* mat; int stride, blk, ebox;
      if (s < 4) { mat = wq; stride = 8192; blk = 4 * g + s; ebox = blk; }
      else { const int e = s - 4; const int m = e < 8 ? (e >> 2) : (g >> 3); blk = e < 8 ? 4 * g + (e & 3) : 64 + (g & 7);
        mat = m ? w3 : w1; stride = 9216; ebox = 64 + 72 * m + blk; }
      const rsrc_t re = mk_rsrc(mat, DM * stride);
      const int voff = r * stride + c8 * 16, sbase = 144 * w * stride + blk * 128;
      v4u e[18];
#pragma unroll
      for (int t = 0; t < 18; ++t) e[t] = ld16(re, voff, sbase + 8 * t * stride);
      SB();
      STAMP(1);
      BAR1();
      tag = MKTAG();
      u32 xv[3], xm[3];
#pragma unroll
      for (int u = 0; u < 3; ++u) { xv[u] = *(LAS u32*)(lds + L_XN + (144 * w + 64 * u + 8 * c8 + r) * 4); xm[u] = dppu<HMIR>(xv[u]); }
      f4 acc = {0.f, 0.f, 0.f, 0.f};
      sfor<0, 18>([&](auto T) { constexpr int t = T.value; acc = mfma(onehot<(t & 7)>(xv[t >> 3], xm[t >> 3], mask), e[t], acc); });
      STAMP(2);
      if (dvalid) *(LAS f4*)(lds + L_RED + w * 512 + dpos) = acc;
      FENCE();
      const u32 old = __builtin_amdgcn_readfirstlane(__hip_atomic_fetch_add((LAS u32*)(lds + L_CNT + lane * 4), 1u, __ATOMIC_RELAXED, __HIP_MEMORY_SCOPE_WORKGROUP));
      FENCE();
      if (old == 3 && lane < 32) {
        f2 a = {0.f, 0.f};
#pragma unroll
        for (int k = 0; k < 8; ++k) a += *(LAS f2*)(lds + L_RED + k * 256 + lane * 8);
        st8c(v2u{pkbf(a.x, a.y), tag}, rw, lane * 8, WS_EBOX + ebox * 256);
      }
      STAMP(3);
      int rho, cb;
      if (s >= 4) { rho = g; cb = s - 4; }
      else { const int qi = s * 16 + g; rho = qi < 18 ? 16 + (qi >= 9) : -1; cb = qi >= 9 ? qi - 9 : qi; }
      if (rho >= 0) {
        _Pragma("nounroll") for (int sp = 0; sp < SPIN_CAP && __builtin_amdgcn_readfirstlane(cnt[192 + w]) == 0u; ++sp) __builtin_amdgcn_s_sleep(1);
        FENCE();
        STAMP(4);
        const int hoff = (4 * c8 + (r >> 1)) * 8, hs = WS_EBOX + (64 + 4 * rho + w) * 256;
        v2u ga = ld8c(rw, hoff, hs), up = ld8c(rw, hoff, hs + 72 * 256);
        const rsrc_t rl = mk_rsrc(w2, FF * 1152);
        const int lo = r * 1152 + c8 * 16, ls = (256 * rho + 64 * w) * 1152 + cb * 128;
        v4u lw[8];
#pragma unroll
        for (int t = 0; t < 8; ++t) lw[t] = ld16(rl, lo, ls + t * 9216);
        SB();
        STAMP(5);
        if (!__all(ga.y == tag && up.y == tag)) {
          _Pragma("nounroll") for (int sp = 0; sp < SPIN_CAP; ++sp) { FENCE(); ga = ld8c(rw, hoff, hs); up = ld8c(rw, hoff, hs + 72 * 256); if (__all(ga.y == tag && up.y == tag)) break; }
        }
        STAMP(6);
        const float gt = asf((r & 1) ? (ga.x & 0xFFFF0000u) : (ga.x << 16)), uv = asf((r & 1) ? (up.x & 0xFFFF0000u) : (up.x << 16));
        const float hv = rbf(gt / (1.f + __expf(-gt))) * uv;
        const u32 hp = pkbf(hv, hv), hm = dppu<HMIR>(hp);
        f4 a2 = {0.f, 0.f, 0.f, 0.f};
        sfor<0, 8>([&](auto T) { constexpr int t = T.value; a2 = mfma(onehot<t>(hp, hm, mask), lw[t], a2); });
        STAMP(7);
        if (dvalid) *(LAS f4*)(lds + L_RED + 2048 + w * 512 + dpos) = a2;
        BAR();
        if (w == 0) {
          float a = 0.f;
#pragma unroll
          for (int k = 0; k < 8; ++k) a += *(LAS float*)(lds + L_RED + 2048 + k * 256 + lane * 4);
          st8c(v2u{asu(a), tag}, rw, (lane >> 4) * (36 * 128) + (lane & 15) * 8, WS_FBOX + (cb * 4 * 36 + 16 + rho) * 128);
        }
        STAMP(8);
      } else {
        const int kap = s * 16 + g - 18;
        if (kap < 36 && w == 0) {
          const u32 xr = *(const GAS unsigned short*)(x + (kap * 16 + (lane & 15)) * 2);
          BAR();
          STAMP(4);
          const int fo = lane * 8, fs = WS_FBOX + kap * 36 * 128;
          v2u m[9];
          _Pragma("nounroll") for (int sp = 0; sp < SPIN_CAP; ++sp) {
            FENCE();
#pragma unroll
            for (int u = 0; u < 8; ++u) m[u] = ld8c(rw, fo + u * 512, fs);
            m[8] = ld8c(rw, (lane & 31) * 8, fs + 8 * 512);
            bool ok = true;
#pragma unroll
            for (int u = 0; u < 9; ++u) ok &= m[u].y == tag;
            if (__all(ok)) break;
          }
          STAMP(5);
          float sv = (asf(m[0].x) + asf(m[1].x)) + (asf(m[2].x) + asf(m[3].x));
          float fv = (asf(m[4].x) + asf(m[5].x)) + (asf(m[6].x) + asf(m[7].x)) + (lane < 32 ? asf(m[8].x) : 0.f);
          sv = rbf(x32_sum(x16_sum(sv))); fv = rbf(x32_sum(x16_sum(fv)));
          const float gg = rbf(fv * __builtin_amdgcn_rcpf(1.f + __expf(-fv)));
          const float o = asf(xr << 16) + rbf(sv * gg);
          if (lane < 16) *(GAS unsigned short*)(out + (kap * 16 + lane) * 2) = (unsigned short)pkbf(o, o);
          if (kap == 0 && lane == 0) __hip_atomic_store((GAS u32*)ws, tag, __ATOMIC_RELAXED, __HIP_MEMORY_SCOPE_AGENT);
          STAMP(6);
        }
      }
    } else {
      BAR1();
      tag = MKTAG();
      _Pragma("nounroll") for (int sp = 0; sp < SPIN_CAP && __builtin_amdgcn_readfirstlane(cnt[192 + w]) == 0u; ++sp) __builtin_amdgcn_s_sleep(1);
      FENCE();
      STAMP(4);
      const int h = w >> 1, half = w & 1, th = s - 13;
      const rsrc_t rl = mk_rsrc(wo, 4096 * 1152);
      const int lo = (32 * (r & 3) + 4 * (r >> 2)) * 1152 + c8 * 16, ls = (256 * g + 128 * h + 2 * half) * 1152 + th * 384;
      v4u lw[24];
#pragma unroll
      for (int t = 0; t < 8; ++t)
#pragma unroll
        for (int k = 0; k < 3; ++k) lw[3 * t + k] = ld16(rl, lo + k * 128, ls + (8 * (t >> 1) + (t & 1)) * 1152);
      SB();
      STAMP(5);
      v4u ml;
      _Pragma("nounroll") for (int sp = 0; sp < SPIN_CAP; ++sp) { FENCE(); ml = ld16c(rw, lane * 16, WS_ML + (2 * g + h) * 1024); if (__all(ml.y == tag && ml.w == tag)) break; }
      float mx = max8(asf(ml.x)); mx = fmaxf(mx, dppf<ROR8>(mx));
      const float ex = __builtin_amdgcn_exp2f((asf(ml.x) - mx) * 1.4426950408889634f);
      const float wt = ex / sum64(ex * asf(ml.z));
      STAMP(6);
      v2u ov[16];
      const int os = WS_OBOX + g * 32768 + half * 1024 + h * 512;
      _Pragma("nounroll") for (int sp = 0; sp < SPIN_CAP; ++sp) {
        FENCE();
        bool ok = true;
#pragma unroll
        for (int k = 0; k < 16; ++k) ov[k] = ld8c(rw, lane * 8, os + k * 2048);
#pragma unroll
        for (int k = 0; k < 16; ++k) ok &= ov[k].y == tag;
        if (__all(ok)) break;
      }
      STAMP(7);
      float o0 = 0.f, o1 = 0.f;
      sfor<0, 8>([&](auto K) { constexpr int k = K.value; o0 = fmaf(dppf<0x150 + 2 * k>(wt), asf(ov[2 * k].x), o0); o1 = fmaf(dppf<0x151 + 2 * k>(wt), asf(ov[2 * k + 1].x), o1); });
      const float of = o0 + o1;
      const u32 op = pkbf(of, of), om = dppu<HMIR>(op);
      f4 a0 = {0.f, 0.f, 0.f, 0.f}, a1 = a0, a2 = a0;
      sfor<0, 8>([&](auto T) { constexpr int t = T.value; const v4u A = onehot<t>(op, om, mask);
        a0 = mfma(A, lw[3 * t], a0); a1 = mfma(A, lw[3 * t + 1], a1); a2 = mfma(A, lw[3 * t + 2], a2); });
      STAMP(8);
      if (dvalid) { *(LAS f4*)(lds + L_RED + w * 512 + dpos) = a0; *(LAS f4*)(lds + L_RED + 2048 + w * 512 + dpos) = a1; *(LAS f4*)(lds + L_RED + 4096 + w * 512 + dpos) = a2; }
      BAR();
      if (w < 3) {
        float a = 0.f;
#pragma unroll
        for (int k = 0; k < 8; ++k) a += *(LAS float*)(lds + L_RED + w * 2048 + k * 256 + lane * 4);
        st8c(v2u{asu(a), tag}, rw, (lane >> 4) * (36 * 128) + (lane & 15) * 8, WS_FBOX + ((3 * th + w) * 4 * 36 + g) * 128);
      }
      STAMP(9);
    }
  } else {
    if (w == 0) {
      if (s < 13) {
        const v4u xa = *(const GAS v4u*)(x + lane * 16);
        const u32 xb = *(const GAS unsigned short*)(x + 1024 + lane * 2);
        float v[9];
        { u32 t0_ = xa.x ^ xb; asm volatile("" : "+v"(t0_)); STAMP(10); }
#pragma unroll
        for (int k = 0; k < 4; ++k) { v[2 * k] = asf(xa[k] << 16); v[2 * k + 1] = asf(xa[k] & 0xFFFF0000u); }
        v[8] = asf(xb << 16);
        float sm = 0.f;
#pragma unroll
        for (int k = 0; k < 9; ++k) sm += v[k];
        const float mean = sum64(sm) * (1.f / DM), mb = rbf(mean);
        float sq = 0.f;
#pragma unroll
        for (int k = 0; k < 9; ++k) { const float d = v[k] - mean; sq = fmaf(d, d, sq); }
        const float sd = rbf(sqrtf(rbf(rbf(sum64(sq) * (1.f / DM)) + 1e-5f)));
        const float rs = 1.f / sd;
        u32 n[9];
#pragma unroll
        for (int k = 0; k < 9; ++k) { const float a = rbf(v[k] - mb), q0 = a * rs, y = fmaf(fmaf(-q0, sd, a), rs, q0); n[k] = pkbf(y, y); }
        *(LAS v4u*)(lds + L_XN + lane * 32) = v4u{n[0], n[1], n[2], n[3]};
        *(LAS v4u*)(lds + L_XN + lane * 32 + 16) = v4u{n[4], n[5], n[6], n[7]};
        *(LAS u32*)(lds + L_XN + 2048 + lane * 4) = n[8];
      }
    } else if (w == 1) {
      *(LAS v4u*)(lds + L_CNT + lane * 16) = v4u{0u, 0u, 0u, 0u};
    }
    const size_t hb = ((size_t)g * SK + (SK - WIN)) * 256;
    const rsrc_t rk = mk_rsrc(kc + hb, WIN * 256), rv = mk_rsrc(vc + hb, WIN * 256);
    const int kb = (s * 256 + w * 64) * 256, rot = ((g + 3 * s) & 15) * 1024, vo = lane * 16;
    STAMP(1);
    if (s < 13) BAR1();
    STAMP(2);
    v4u K[16], V[16], q[4];
#pragma unroll
    for (int t = 0; t < 16; ++t) K[t] = ld16(rk, vo, kb + ((t * 1024) ^ rot));
    if (s >= 13) BAR1();
    const u32 tag = MKTAG();
    STAMP(3);
#pragma unroll
    for (int t = 0; t < 6; ++t) V[t] = ld16(rv, vo, kb + ((t * 1024) ^ rot));
    const int qo = (lane & 15) * 32, qs = WS_EBOX + g * 1024;
    FENCE();
#pragma unroll
    for (int k = 0; k < 4; ++k) q[k] = ld16c(rw, qo + (k & 1) * 16 + (k >> 1) * 512, qs);
    FENCE();
#pragma unroll
    for (int t = 6; t < 16; ++t) V[t] = ld16(rv, vo, kb + ((t * 1024) ^ rot));
    SB();
    FENCE();
    cnt[192 + w] = 1u;
    STAMP(4);
    auto qok = [&]() { bool ok = true;
#pragma unroll
      for (int k = 0; k < 4; ++k) ok &= (q[k].y == tag) & (q[k].w == tag);
      return __all(ok); };
    if (!qok()) {
      _Pragma("nounroll") for (int sp = 0; sp < SPIN_CAP; ++sp) { FENCE();
#pragma unroll
        for (int k = 0; k < 4; ++k) q[k] = ld16c(rw, qo + (k & 1) * 16 + (k >> 1) * 512, qs);
        if (qok()) break; }
    }
    STAMP(5);
    float sc[2];
#pragma unroll
    for (int hf = 0; hf < 2; ++hf) {
      float y[16];
#pragma unroll
      for (int t = 0; t < 8; ++t)
#pragma unroll
        for (int h = 0; h < 2; ++h) { const v4u kk = K[8 * hf + t];
          y[8 * h + t] = dot2(kk.w, q[2 * h + 1].z, dot2(kk.z, q[2 * h + 1].x, dot2(kk.y, q[2 * h].z, dot2(kk.x, q[2 * h].x, 0.f)))); }
#define TRS(H, C) _Pragma("unroll") for (int j = 0; j < (H); ++j) { const float lo_ = y[j] + dppf<C>(y[j]), hi_ = y[j + (H)] + dppf<C>(y[j + (H)]); y[j] = (lane & (H)) ? hi_ : lo_; }
      TRS(8, ROR8) TRS(4, HMIR) TRS(2, XOR2) TRS(1, XOR1)
#undef TRS
      sc[hf] = rbf(rbf(y[0]) * 0.08838834764831845f);
    }
    const int h = r & 1;
    float mx = x32_max(x16_max(max8(fmaxf(sc[0], sc[1]))));
    *(volatile LAS float*)(lds + L_M + h * 16 + w * 4) = mx;
    FENCE();
    __hip_atomic_fetch_add((LAS u32*)(lds + L_CNT + 256 + lane * 4), 1u, __ATOMIC_RELAXED, __HIP_MEMORY_SCOPE_WORKGROUP);
    FENCE();
    STAMP(6);
    _Pragma("nounroll") for (int sp = 0; sp < SPIN_CAP && __builtin_amdgcn_readfirstlane(cnt[64]) != 4u; ++sp) {}
    FENCE();
    { const f4 mm = *(LAS f4*)(lds + L_M + h * 16); mx = fmaxf(fmaxf(mm.x, mm.y), fmaxf(mm.z, mm.w)); }
    const float p0 = __builtin_amdgcn_exp2f((sc[0] - mx) * 1.4426950408889634f), p1 = __builtin_amdgcn_exp2f((sc[1] - mx) * 1.4426950408889634f);
    const float ls = x32_sum(x16_sum(sum8(p0 + p1)));
    st16c(v4u{asu(mx), tag, asu(ls), tag}, rw, (lane & 55) == 0 ? h * 1024 : OOB, WS_ML + g * 2048 + w * 256 + s * 16);
    u32 pp[2] = {pkbf(p0, p0), pkbf(p1, p1)}, pm[2] = {dppu<HMIR>(pp[0]), dppu<HMIR>(pp[1])};
    STAMP(7);
    f4 acc = {0.f, 0.f, 0.f, 0.f};
    sfor<0, 16>([&](auto T) { constexpr int t = T.value; acc = mfma(onehot<(t & 7)>(pp[t >> 3], pm[t >> 3], mask), V[t], acc); });
    STAMP(8);
    *(LAS f4*)(lds + L_ORED + w * 1024 + lane * 16) = acc;
    FENCE();
    const u32 old = __builtin_amdgcn_readfirstlane(__hip_atomic_fetch_add((LAS u32*)(lds + L_CNT + 512 + lane * 4), 1u, __ATOMIC_RELAXED, __HIP_MEMORY_SCOPE_WORKGROUP));
    FENCE();
    if (old == 3) {
      const f4 a0 = *(LAS f4*)(lds + L_ORED + lane * 16), a1 = *(LAS f4*)(lds + L_ORED + 1024 + lane * 16), a2 = *(LAS f4*)(lds + L_ORED + 2048 + lane * 16), a3 = *(LAS f4*)(lds + L_ORED + 3072 + lane * 16);
      const f4 a = (a0 + a1) + (a2 + a3);
      const int os = WS_OBOX + (g * 16 + s) * 2048;
      st16c(v4u{asu(a.x), tag, asu(a.y), tag}, rw, vo, os);
      st16c(v4u{asu(a.z), tag, asu(a.w), tag}, rw, vo + 1024, os);
    }
    STAMP(9);
  }
  STAMP_OUT();
}

extern "C" void kernel_launch(void* const* d_in, const int* in_sizes, int n_in, void* d_out, int out_size, void* d_ws, size_t ws_size, hipStream_t stream) {
  (void)hipFuncSetAttribute((const void*)fused_decode, hipFuncAttributeMaxDynamicSharedMemorySize, LDS_BYTES);
  fused_decode<<<256, 512, LDS_BYTES, stream>>>((const char*)d_in[0], (const char*)d_in[1], (const char*)d_in[2], (const char*)d_in[3], (const char*)d_in[4],
                                                (const char*)d_in[6], (const char*)d_in[7], (const char*)d_in[8], (char*)d_out, (char*)d_ws);
}
```

```cpp
#include <hip/hip_runtime.h>
#include <stdint.h>

typedef unsigned u32;
typedef unsigned long long u64;
typedef u32 v4u __attribute__((ext_vector_type(4)));
typedef u32 v2u __attribute__((ext_vector_type(2)));
typedef float f4 __attribute__((ext_vector_type(4)));
typedef float f2 __attribute__((ext_vector_type(2)));
typedef __bf16 bf8 __attribute__((ext_vector_type(8)));
typedef __bf16 bf2 __attribute__((ext_vector_type(2)));
#define LAS __attribute__((address_space(3)))
#define GAS __attribute__((address_space(1)))
#define CAS __attribute__((address_space(4)))
typedef __amdgpu_buffer_rsrc_t rsrc_t;

#define STAMPS 1
#define PROBE 0

constexpr int DM = 576, SK = 65536, WIN = 4096, FF = 4608;
constexpr int AUX_NT = 2, AUX_SC1 = 16;
constexpr int WS_EBOX = 4096;
constexpr int WS_ML = 65536;
constexpr int WS_OBOX = 131072;
constexpr int WS_FBOX = 131072 + 524288;
constexpr int WS_END = WS_FBOX + 36 * 36 * 128;
constexpr int WS_STAMP = 1 << 20;
constexpr int SPIN_CAP = 1 << 18;
constexpr int L_XN = 0;
constexpr int L_RED = 2560;
constexpr int L_ORED = 8704;
constexpr int L_M = 12800;
constexpr int L_CNT = 13056;
constexpr int L_P = 14336;
#define WE 5
#define WK 5
#define WV 5
#define WL 5
#define WF 7
constexpr int LDS_BYTES = 96 * 1024;

__device__ __forceinline__ rsrc_t mk_rsrc(const void* p, u32 bytes) { return __builtin_amdgcn_make_buffer_rsrc((void*)p, 0, bytes, 0x00020000); }
__device__ __forceinline__ v4u ld16(rsrc_t r, int v, int s) { return __builtin_amdgcn_raw_buffer_load_b128(r, v, s, AUX_NT); }
__device__ __forceinline__ v4u ld16c(rsrc_t r, int v, int s) { return __builtin_amdgcn_raw_buffer_load_b128(r, v, s, AUX_SC1); }
__device__ __forceinline__ v2u ld8c(rsrc_t r, int v, int s) { return __builtin_amdgcn_raw_buffer_load_b64(r, v, s, AUX_SC1); }
__device__ __forceinline__ void st16c(v4u x, rsrc_t r, int v, int s) { __builtin_amdgcn_raw_buffer_store_b128(x, r, v + s, 0, AUX_SC1); }
__device__ __forceinline__ void st8c(v2u x, rsrc_t r, int v, int s) { __builtin_amdgcn_raw_buffer_store_b64(x, r, v, s, AUX_SC1); }
__device__ __forceinline__ float asf(u32 x) { return __builtin_bit_cast(float, x); }
__device__ __forceinline__ u32 asu(float x) { return __builtin_bit_cast(u32, x); }
__device__ __forceinline__ u32 pkbf(float lo, float hi) { bf2 r = {(__bf16)lo, (__bf16)hi}; return __builtin_bit_cast(u32, r); }
__device__ __forceinline__ float rbf(float x) { return asf(pkbf(x, x) << 16); }
template <int C> __device__ __forceinline__ u32 dppu(u32 x) { return (u32)__builtin_amdgcn_mov_dpp((int)x, C, 0xf, 0xf, true); }
template <int C> __device__ __forceinline__ float dppf(float x) { return asf(dppu<C>(asu(x))); }
constexpr int XOR1 = 0xB1, XOR2 = 0x4E, HMIR = 0x141, ROR8 = 0x128;
__device__ __forceinline__ float x16_sum(float x) { auto s = __builtin_amdgcn_permlane16_swap(asu(x), asu(x), false, false); return asf(s[0]) + asf(s[1]); }
__device__ __forceinline__ float x32_sum(float x) { auto s = __builtin_amdgcn_permlane32_swap(asu(x), asu(x), false, false); return asf(s[0]) + asf(s[1]); }
__device__ __forceinline__ float x16_max(float x) { auto s = __builtin_amdgcn_permlane16_swap(asu(x), asu(x), false, false); return fmaxf(asf(s[0]), asf(s[1])); }
__device__ __forceinline__ float x32_max(float x) { auto s = __builtin_amdgcn_permlane32_swap(asu(x), asu(x), false, false); return fmaxf(asf(s[0]), asf(s[1])); }
__device__ __forceinline__ float sum8(float x) { x += dppf<XOR1>(x); x += dppf<XOR2>(x); x += dppf<HMIR>(x); return x; }
__device__ __forceinline__ float max8(float x) { x = fmaxf(x, dppf<XOR1>(x)); x = fmaxf(x, dppf<XOR2>(x)); x = fmaxf(x, dppf<HMIR>(x)); return x; }
__device__ __forceinline__ float sum64(float x) { x = sum8(x); x += dppf<ROR8>(x); return x32_sum(x16_sum(x)); }
__device__ __forceinline__ float dot2(u32 a, u32 b, float c) { return __builtin_amdgcn_fdot2_f32_bf16(__builtin_bit_cast(bf2, a), __builtin_bit_cast(bf2, b), c, false); }
__device__ __forceinline__ f4 mfma(v4u a, v4u b, f4 c) { return __builtin_amdgcn_mfma_f32_16x16x32_bf16(__builtin_bit_cast(bf8, a), __builtin_bit_cast(bf8, b), c, 0, 0, 0); }
template <int T> __device__ __forceinline__ v4u onehot(u32 p, u32 pm, const v4u& mask) {
  constexpr int a = (T & 3) * 0x55, b = (3 - (T & 3)) * 0x55;
  v4u r;
  if (T < 4) { r.x = dppu<a>(p) & mask.x; r.y = dppu<a>(p) & mask.y; r.z = dppu<b>(pm) & mask.z; r.w = dppu<b>(pm) & mask.w; }
  else { r.x = dppu<b>(pm) & mask.x; r.y = dppu<b>(pm) & mask.y; r.z = dppu<a>(p) & mask.z; r.w = dppu<a>(p) & mask.w; }
  return r;
}
#define FENCE() asm volatile("" ::: "memory")
#define SB() __builtin_amdgcn_sched_barrier(0)
#define VMW(n) asm volatile("s_waitcnt vmcnt(%0)" :: "i"(n) : "memory")
#define PACE(t, W) do { if ((t) >= (W)) VMW((W) - 1); } while (0)
#define PACE2(t, W, fast) do { if ((t) >= WF) VMW(WF - 1); if ((t) >= (W) && !(fast)) VMW((W) - 1); } while (0)
#define WAITFLAG(i) do { _Pragma("nounroll") for (int sp = 0; sp < SPIN_CAP && __builtin_amdgcn_readfirstlane(cnt[256 + (i)]) == 0u; ++sp) __builtin_amdgcn_s_sleep(1); FENCE(); } while (0)
#define SETFLAG(i) do { FENCE(); cnt[256 + (i)] = 1u; FENCE(); } while (0)
#define ARRIVE(row) __builtin_amdgcn_readfirstlane(__hip_atomic_fetch_add((LAS u32*)(lds + L_CNT + (row) * 256 + lane * 4), 1u, __ATOMIC_RELAXED, __HIP_MEMORY_SCOPE_WORKGROUP))
#define LGKM0() asm volatile("s_waitcnt lgkmcnt(0)" ::: "memory")
#define BAR() do { LGKM0(); __builtin_amdgcn_s_barrier(); FENCE(); } while (0)
#define BAR1() do { asm volatile("s_waitcnt lgkmcnt(0)\n\ts_barrier" ::: "memory"); } while (0)
#define MKTAG() ({ u32 t_; asm volatile("s_mul_i32 %0, %1, 0x19660d\n\ts_add_u32 %0, %0, 0x3c6ef35f" : "=s"(t_) : "s"(epoch) : "scc"); t_; })
constexpr int OOB = 0x70000000;
template <int I, int N, class F> __device__ __forceinline__ void sfor(F&& f) { if constexpr (I < N) { f(std::integral_constant<int, I>{}); sfor<I + 1, N>(f); } }

#define STAMP(i) do { SB(); ts[i] = (u32)__builtin_amdgcn_s_memrealtime(); SB(); } while (0)
#define STAMP_OUT() do { if (lane == 0) { GAS u32* o_ = (GAS u32*)(ws + WS_STAMP) + (blockIdx.x * 12 + wave) * 16; _Pragma("unroll") for (int i_ = 0; i_ < 12; ++i_) o_[i_] = ts[i_]; o_[15] = __builtin_amdgcn_s_getreg((3 << 11) | 20) & 15; } } while (0)

__global__ void __launch_bounds__(768, 3)
fused_decode(const char* x, const char* kc, const char* vc, const char* wq, const char* wo, const char* w1, const char* w3, const char* w2, char* out, char* ws) {
  extern __shared__ __attribute__((aligned(16))) char lds_[];
  LAS char* lds = (LAS char*)lds_;
  volatile LAS u32* cnt = (volatile LAS u32*)(lds + L_CNT);
  const int tid = threadIdx.x, lane = tid & 63, wave = __builtin_amdgcn_readfirstlane(tid >> 6), w = wave & 3;
  const int b = blockIdx.x, g = b & 15, s = b >> 4;
  u32 ts[12] = {0, 0, 0, 0, 0, 0, 0, 0, 0, 0, 0, 0};
  STAMP(0);
  u32 epoch;
  asm volatile("s_load_dword %0, %10, 0x0" : "=&s"(epoch), "+s"(x), "+s"(kc), "+s"(vc), "+s"(wq), "+s"(wo), "+s"(w1), "+s"(w3), "+s"(w2), "+s"(out), "+s"(ws));
  const rsrc_t rw = mk_rsrc(ws, WS_END);
  const int r = lane >> 3, c8 = lane & 7;
  v4u mask; { const u32 hm = (lane & 1) ? 0xFFFF0000u : 0x0000FFFFu; const int q = c8 >> 1;
    mask.x = q == 0 ? hm : 0u; mask.y = q == 1 ? hm : 0u; mask.z = q == 2 ? hm : 0u; mask.w = q == 3 ? hm : 0u; }
  const bool dvalid = (lane >> 5) == (r & 1);
  const int dpos = ((lane >> 5) * 64 + c8 * 8 + ((lane >> 4) & 1) * 4) * 4;

  if (wave < 4) {
    u32 tag;
    if (s < 13) {
      const char* mat; int stride, blk, ebox;
      if (s < 4) { mat = wq; stride = 8192; blk = 4 * g + s; ebox = blk; }
      else { const int e = s - 4; const int m = e < 8 ? (e >> 2) : (g >> 3); blk = e < 8 ? 4 * g + (e & 3) : 64 + (g & 7);
        mat = m ? w3 : w1; stride = 9216; ebox = 64 + 72 * m + blk; }
      const rsrc_t re = mk_rsrc(mat, DM * stride);
      const int voff = r * stride + c8 * 16, sbase = 144 * w * stride + blk * 128;
      v4u e[18];
#pragma unroll
      for (int t = 0; t < 18; ++t) { PACE(t, WE); e[t] = ld16(re, voff, sbase + 8 * t * stride); }
      SB();
      STAMP(1);
      BAR1();
      tag = MKTAG();
      u32 xv[3], xm[3];
#pragma unroll
      for (int u = 0; u < 3; ++u) { xv[u] = *(LAS u32*)(lds + L_XN + (144 * w + 64 * u + 8 * c8 + r) * 4); xm[u] = dppu<HMIR>(xv[u]); }
      f4 acc = {0.f, 0.f, 0.f, 0.f};
      sfor<0, 18>([&](auto T) { constexpr int t = T.value; acc = mfma(onehot<(t & 7)>(xv[t >> 3], xm[t >> 3], mask), e[t], acc); });
      STAMP(2);
      if (dvalid) *(LAS f4*)(lds + L_RED + w * 512 + dpos) = acc;
      FENCE();
      const u32 old = ARRIVE(0);
      FENCE();
      if (old == 3 && lane < 32) {
        f2 a = {0.f, 0.f};
#pragma unroll
        for (int k = 0; k < 8; ++k) a += *(LAS f2*)(lds + L_RED + k * 256 + lane * 8);
        st8c(v2u{pkbf(a.x, a.y), tag}, rw, lane * 8, WS_EBOX + ebox * 256);
      }
      STAMP(3);
      int rho, cb;
      if (s >= 4) { rho = g; cb = s - 4; }
      else { const int qi = s * 16 + g; rho = qi < 18 ? 16 + (qi >= 9) : -1; cb = qi >= 9 ? qi - 9 : qi; }
      if (rho >= 0) {
        WAITFLAG(4 + w);
        STAMP(4);
        const int hoff = (4 * c8 + (r >> 1)) * 8, hs = WS_EBOX + (64 + 4 * rho + w) * 256;
        v2u ga = ld8c(rw, hoff, hs), up = ld8c(rw, hoff, hs + 72 * 256);
        const rsrc_t rl = mk_rsrc(w2, FF * 1152);
        const int lo = r * 1152 + c8 * 16, ls = (256 * rho + 64 * w) * 1152 + cb * 128;
        v4u lw[8];
#pragma unroll
        for (int t = 0; t < 8; ++t) { PACE(t, WL); lw[t] = ld16(rl, lo, ls + t * 9216); }
        SB();
        STAMP(5);
        if (!__all(ga.y == tag && up.y == tag)) {
          _Pragma("nounroll") for (int sp = 0; sp < SPIN_CAP; ++sp) { FENCE(); ga = ld8c(rw, hoff, hs); up = ld8c(rw, hoff, hs + 72 * 256); if (__all(ga.y == tag && up.y == tag)) break; }
        }
        STAMP(6);
        const float gt = asf((r & 1) ? (ga.x & 0xFFFF0000u) : (ga.x << 16)), uv = asf((r & 1) ? (up.x & 0xFFFF0000u) : (up.x << 16));
        const float hv = rbf(gt / (1.f + __expf(-gt))) * uv;
        const u32 hp = pkbf(hv, hv), hm = dppu<HMIR>(hp);
        f4 a2 = {0.f, 0.f, 0.f, 0.f};
        sfor<0, 8>([&](auto T) { constexpr int t = T.value; a2 = mfma(onehot<t>(hp, hm, mask), lw[t], a2); });
        STAMP(7);
        if (dvalid) *(LAS f4*)(lds + L_RED + 2048 + w * 512 + dpos) = a2;
        BAR();
        if (w == 0) {
          float a = 0.f;
#pragma unroll
          for (int k = 0; k < 8; ++k) a += *(LAS float*)(lds + L_RED + 2048 + k * 256 + lane * 4);
          st8c(v2u{asu(a), tag}, rw, (lane >> 4) * (36 * 128) + (lane & 15) * 8, WS_FBOX + (cb * 4 * 36 + 16 + rho) * 128);
        }
        STAMP(8);
      } else {
        const int kap = s * 16 + g - 18;
        if (kap < 36 && w == 0) {
          const u32 xr = *(const GAS unsigned short*)(x + (kap * 16 + (lane & 15)) * 2);
          BAR();
          STAMP(4);
          const int fo = lane * 8, fs = WS_FBOX + kap * 36 * 128;
          v2u m[9];
          _Pragma("nounroll") for (int sp = 0; sp < SPIN_CAP; ++sp) {
            FENCE();
#pragma unroll
            for (int u = 0; u < 8; ++u) m[u] = ld8c(rw, fo + u * 512, fs);
            m[8] = ld8c(rw, (lane & 31) * 8, fs + 8 * 512);
            bool ok = true;
#pragma unroll
            for (int u = 0; u < 9; ++u) ok &= m[u].y == tag;
            if (__all(ok)) break;
          }
          STAMP(5);
          float sv = (asf(m[0].x) + asf(m[1].x)) + (asf(m[2].x) + asf(m[3].x));
          float fv = (asf(m[4].x) + asf(m[5].x)) + (asf(m[6].x) + asf(m[7].x)) + (lane < 32 ? asf(m[8].x) : 0.f);
          sv = rbf(x32_sum(x16_sum(sv))); fv = rbf(x32_sum(x16_sum(fv)));
          const float gg = rbf(fv * __builtin_amdgcn_rcpf(1.f + __expf(-fv)));
          const float o = asf(xr << 16) + rbf(sv * gg);
          if (lane < 16) *(GAS unsigned short*)(out + (kap * 16 + lane) * 2) = (unsigned short)pkbf(o, o);
          if (kap == 0 && lane == 0) __hip_atomic_store((GAS u32*)ws, tag, __ATOMIC_RELAXED, __HIP_MEMORY_SCOPE_AGENT);
          STAMP(6);
          if (kap == 0) { const u32 tgt = 4000u + 10u * (ts[PROBE] - ts[0]); _Pragma("nounroll") for (int sp = 0; sp < (1 << 20) && (u32)__builtin_amdgcn_s_memrealtime() - ts[0] < tgt; ++sp) __builtin_amdgcn_s_sleep(1); }
        }
      }
    } else {
      BAR1();
      tag = MKTAG();
      WAITFLAG(4 + w);
      STAMP(4);
      const int h = w >> 1, half = w & 1, th = s - 13;
      const rsrc_t rl = mk_rsrc(wo, 4096 * 1152);
      const int lo = (32 * (r & 3) + 4 * (r >> 2)) * 1152 + c8 * 16, ls = (256 * g + 128 * h + 2 * half) * 1152 + th * 384;
      v4u lw[24];
#pragma unroll
      for (int t = 0; t < 8; ++t)
#pragma unroll
        for (int k = 0; k < 3; ++k) { PACE(3 * t + k, WF); lw[3 * t + k] = ld16(rl, lo + k * 128, ls + (8 * (t >> 1) + (t & 1)) * 1152); }
      SB();
      STAMP(5);
      v4u ml;
      _Pragma("nounroll") for (int sp = 0; sp < SPIN_CAP; ++sp) { FENCE(); ml = ld16c(rw, (lane & 15) * 128, WS_ML + g * 2048 + h * 16); if (__all(ml.y == tag && ml.w == tag)) break; }
      float mx = max8(asf(ml.x)); mx = fmaxf(mx, dppf<ROR8>(mx));
      const float ex = __builtin_amdgcn_exp2f((asf(ml.x) - mx) * 1.4426950408889634f);
      float lsum = sum8(ex * asf(ml.z)); lsum += dppf<ROR8>(lsum);
      const float wt = ex / lsum;
      STAMP(6);
      v2u ov[16];
      const int os = WS_OBOX + g * 32768 + half * 1024 + h * 512;
      _Pragma("nounroll") for (int sp = 0; sp < SPIN_CAP; ++sp) {
        FENCE();
        bool ok = true;
#pragma unroll
        for (int k = 0; k < 16; ++k) ov[k] = ld8c(rw, lane * 8, os + k * 2048);
#pragma unroll
        for (int k = 0; k < 16; ++k) ok &= ov[k].y == tag;
        if (__all(ok)) break;
      }
      STAMP(7);
      float o0 = 0.f, o1 = 0.f;
      sfor<0, 8>([&](auto K) { constexpr int k = K.value; o0 = fmaf(dppf<0x150 + 2 * k>(wt), asf(ov[2 * k].x), o0); o1 = fmaf(dppf<0x151 + 2 * k>(wt), asf(ov[2 * k + 1].x), o1); });
      const float of = o0 + o1;
      const u32 op = pkbf(of, of), om = dppu<HMIR>(op);
      f4 a0 = {0.f, 0.f, 0.f, 0.f}, a1 = a0, a2 = a0;
      sfor<0, 8>([&](auto T) { constexpr int t = T.value; const v4u A = onehot<t>(op, om, mask);
        a0 = mfma(A, lw[3 * t], a0); a1 = mfma(A, lw[3 * t + 1], a1); a2 = mfma(A, lw[3 * t + 2], a2); });
      STAMP(8);
      if (dvalid) { *(LAS f4*)(lds + L_RED + w * 512 + dpos) = a0; *(LAS f4*)(lds + L_RED + 2048 + w * 512 + dpos) = a1; *(LAS f4*)(lds + L_RED + 4096 + w * 512 + dpos) = a2; }
      BAR();
      if (w < 3) {
        float a = 0.f;
#pragma unroll
        for (int k = 0; k < 8; ++k) a += *(LAS float*)(lds + L_RED + w * 2048 + k * 256 + lane * 4);
        st8c(v2u{asu(a), tag}, rw, (lane >> 4) * (36 * 128) + (lane & 15) * 8, WS_FBOX + ((3 * th + w) * 4 * 36 + g) * 128);
      }
      STAMP(9);
    }
  } else {
    const size_t hb = ((size_t)g * SK + (SK - WIN)) * 256;
    const int kb = (s * 256 + w * 64) * 256, rot = ((g + 3 * s) & 15) * 1024, vo = lane * 16;
    const bool fast = s >= 13;
    if (wave < 8) {
      const rsrc_t rk = mk_rsrc(kc + hb, WIN * 256);
      if (!fast) BAR1();
      STAMP(2);
      v4u K[16], q[4];
#pragma unroll
      for (int t = 0; t < 16; ++t) { PACE2(t, WK, fast); K[t] = ld16(rk, vo, kb + ((t * 1024) ^ rot)); if (t == WK - 1 && fast) BAR1(); }
      SETFLAG(w);
      const u32 tag = MKTAG();
      STAMP(3);
      const int qo = (lane & 15) * 32, qs = WS_EBOX + g * 1024;
      _Pragma("nounroll") for (int sp = 0; sp < SPIN_CAP; ++sp) { FENCE();
        bool ok = true;
#pragma unroll
        for (int k = 0; k < 4; ++k) q[k] = ld16c(rw, qo + (k & 1) * 16 + (k >> 1) * 512, qs);
#pragma unroll
        for (int k = 0; k < 4; ++k) ok &= (q[k].y == tag) & (q[k].w == tag);
        if (__all(ok)) break;
        __builtin_amdgcn_s_sleep(2); }
      STAMP(5);
    float sc[2];
#pragma unroll
    for (int hf = 0; hf < 2; ++hf) {
      float y[16];
#pragma unroll
      for (int t = 0; t < 8; ++t)
#pragma unroll
        for (int h = 0; h < 2; ++h) { const v4u kk = K[8 * hf + t];
          y[8 * h + t] = dot2(kk.w, q[2 * h + 1].z, dot2(kk.z, q[2 * h + 1].x, dot2(kk.y, q[2 * h].z, dot2(kk.x, q[2 * h].x, 0.f)))); }
#define TRS(H, C) _Pragma("unroll") for (int j = 0; j < (H); ++j) { const float lo_ = y[j] + dppf<C>(y[j]), hi_ = y[j + (H)] + dppf<C>(y[j + (H)]); y[j] = (lane & (H)) ? hi_ : lo_; }
      TRS(8, ROR8) TRS(4, HMIR) TRS(2, XOR2) TRS(1, XOR1)
#undef TRS
      sc[hf] = rbf(rbf(y[0]) * 0.08838834764831845f);
    }
    const int h = r & 1;
      float mx = x32_max(x16_max(max8(fmaxf(sc[0], sc[1]))));
      *(volatile LAS float*)(lds + L_M + h * 16 + w * 4) = mx;
      FENCE();
      u32 na = ARRIVE(1) + 1;
      STAMP(6);
      _Pragma("nounroll") for (int sp = 0; sp < SPIN_CAP && na != 4u; ++sp) na = __builtin_amdgcn_readfirstlane(cnt[64]);
      FENCE();
      { const f4 mm = *(LAS f4*)(lds + L_M + h * 16); mx = fmaxf(fmaxf(mm.x, mm.y), fmaxf(mm.z, mm.w)); }
      const float p0 = __builtin_amdgcn_exp2f((sc[0] - mx) * 1.4426950408889634f), p1 = __builtin_amdgcn_exp2f((sc[1] - mx) * 1.4426950408889634f);
      *(LAS v2u*)(lds + L_P + w * 512 + lane * 8) = v2u{pkbf(p0, p0), pkbf(p1, p1)};
      SETFLAG(8 + w);
      STAMP(7);
      const float ls = x32_sum(x16_sum(sum8(p0 + p1)));
      *(volatile LAS float*)(lds + L_M + 32 + h * 16 + w * 4) = ls;
      FENCE();
      const u32 old = ARRIVE(3);
      FENCE();
      if (old == 3) {
        const f4 ll = *(LAS f4*)(lds + L_M + 32 + h * 16);
        st16c(v4u{asu(mx), tag, asu((ll.x + ll.y) + (ll.z + ll.w)), tag}, rw, (lane & 55) == 0 ? h * 16 : OOB, WS_ML + (g * 16 + s) * 128);
      }
      STAMP(8);
    } else {
      if (w == 0) {
        if (!fast) {
        const v4u xa = *(const GAS v4u*)(x + lane * 16);
        const u32 xb = *(const GAS unsigned short*)(x + 1024 + lane * 2);
        float v[9];
        { u32 t0_ = xa.x ^ xb; asm volatile("" : "+v"(t0_)); STAMP(10); }
#pragma unroll
        for (int k = 0; k < 4; ++k) { v[2 * k] = asf(xa[k] << 16); v[2 * k + 1] = asf(xa[k] & 0xFFFF0000u); }
        v[8] = asf(xb << 16);
        float sm = 0.f;
#pragma unroll
        for (int k = 0; k < 9; ++k) sm += v[k];
        const float mean = sum64(sm) * (1.f / DM), mb = rbf(mean);
        float sq = 0.f;
#pragma unroll
        for (int k = 0; k < 9; ++k) { const float d = v[k] - mean; sq = fmaf(d, d, sq); }
        const float sd = rbf(sqrtf(rbf(rbf(sum64(sq) * (1.f / DM)) + 1e-5f)));
        const float rs = 1.f / sd;
        u32 n[9];
#pragma unroll
        for (int k = 0; k < 9; ++k) { const float a = rbf(v[k] - mb), q0 = a * rs, y = fmaf(fmaf(-q0, sd, a), rs, q0); n[k] = pkbf(y, y); }
        *(LAS v4u*)(lds + L_XN + lane * 32) = v4u{n[0], n[1], n[2], n[3]};
        *(LAS v4u*)(lds + L_XN + lane * 32 + 16) = v4u{n[4], n[5], n[6], n[7]};
        *(LAS u32*)(lds + L_XN + 2048 + lane * 4) = n[8];
        }
      } else if (w == 1) {
        *(LAS v4u*)(lds + L_CNT + lane * 16) = v4u{0u, 0u, 0u, 0u};
        *(LAS u32*)(lds + L_CNT + 1024 + lane * 4) = 0u;
      }
      const rsrc_t rv = mk_rsrc(vc + hb, WIN * 256);
      STAMP(1);
      BAR1();
      const u32 tag = MKTAG();
      WAITFLAG(w);
      STAMP(3);
      v4u V[16];
#pragma unroll
      for (int t = 0; t < 16; ++t) { PACE2(t, WV, fast); V[t] = ld16(rv, vo, kb + ((t * 1024) ^ rot)); }
      SETFLAG(4 + w);
      STAMP(4);
      WAITFLAG(8 + w);
      const v2u pp = *(LAS v2u*)(lds + L_P + w * 512 + lane * 8);
      const u32 pm[2] = {dppu<HMIR>(pp.x), dppu<HMIR>(pp.y)};
      STAMP(7);
      f4 acc = {0.f, 0.f, 0.f, 0.f};
      sfor<0, 16>([&](auto T) { constexpr int t = T.value; acc = mfma(onehot<(t & 7)>(pp[t >> 3], pm[t >> 3], mask), V[t], acc); });
      STAMP(8);
      *(LAS f4*)(lds + L_ORED + w * 1024 + lane * 16) = acc;
      FENCE();
      const u32 old = ARRIVE(2);
      FENCE();
      if (old == 3) {
        const f4 a0 = *(LAS f4*)(lds + L_ORED + lane * 16), a1 = *(LAS f4*)(lds + L_ORED + 1024 + lane * 16), a2 = *(LAS f4*)(lds + L_ORED + 2048 + lane * 16), a3 = *(LAS f4*)(lds + L_ORED + 3072 + lane * 16);
        const f4 a = (a0 + a1) + (a2 + a3);
        const int os = WS_OBOX + (g * 16 + s) * 2048;
        st16c(v4u{asu(a.x), tag, asu(a.y), tag}, rw, vo, os);
        st16c(v4u{asu(a.z), tag, asu(a.w), tag}, rw, vo + 1024, os);
      }
      STAMP(9);
    }
  }
}

extern "C" void kernel_launch(void* const* d_in, const int* in_sizes, int n_in, void* d_out, int out_size, void* d_ws, size_t ws_size, hipStream_t stream) {
  (void)hipFuncSetAttribute((const void*)fused_decode, hipFuncAttributeMaxDynamicSharedMemorySize, LDS_BYTES);
  fused_decode<<<256, 768, LDS_BYTES, stream>>>((const char*)d_in[0], (const char*)d_in[1], (const char*)d_in[2], (const char*)d_in[3], (const char*)d_in[4],
                                                (const char*)d_in[6], (const char*)d_in[7], (const char*)d_in[8], (char*)d_out, (char*)d_ws);
}
constexpr int NWAVES = 12, NCLASS = 7;
static int stamp_class(int b, int w) { int s = b >> 4, g = b & 15; if (w >= 8) return s >= 13 ? 6 : 5; if (w >= 4) return s >= 13 ? 4 : 3; if (s >= 13) return 2; if (s < 4 && s * 16 + g >= 18) return 1; return 0; }
static const char* class_name(int c) { static const char* n[] = {"X-w2", "X-red", "X-wo", "Y", "Y-o", "Z", "Z-o"}; return n[c]; }
```

```cpp
#include <hip/hip_runtime.h>
#include <stdint.h>

typedef unsigned u32;
typedef unsigned long long u64;
typedef u32 v4u __attribute__((ext_vector_type(4)));
typedef u32 v2u __attribute__((ext_vector_type(2)));
typedef float f4 __attribute__((ext_vector_type(4)));
typedef float f2 __attribute__((ext_vector_type(2)));
typedef __bf16 bf8 __attribute__((ext_vector_type(8)));
typedef __bf16 bf2 __attribute__((ext_vector_type(2)));
#define LAS __attribute__((address_space(3)))
#define GAS __attribute__((address_space(1)))
#define CAS __attribute__((address_space(4)))
typedef __amdgpu_buffer_rsrc_t rsrc_t;

constexpr int DM = 576, SK = 65536, WIN = 4096, FF = 4608;
constexpr int AUX_NT = 2, AUX_SC1 = 16;
constexpr int WS_BEACON = 1024;
constexpr int WS_QBOX = 49152;
constexpr int WS_HBOX = 53248;
constexpr int WS_ML = 8192;
constexpr int WS_FBOX = 12288;
constexpr int WS_OBOX = 32768;
constexpr int RS = 131072 + 2176;
constexpr int WS_END = 16 * RS;
__device__ __forceinline__ int qbox_off(int g, int m) { return WS_QBOX + ((g + 4 * m) & 15) * RS + m * 1024; }
__device__ __forceinline__ int hbox_off(int j) { return WS_HBOX + (j & 15) * RS + (j >> 4) * 512; }
constexpr int SPIN_CAP = 1 << 18;
constexpr int OOB = 0x70000000;
constexpr int L_XN = 0;
constexpr int L_RED = 2560;
constexpr int L_RED2 = 4608;
constexpr int L_ORED = 10752;
constexpr int L_M = 14848;
constexpr int L_P = 15104;
constexpr int L_CNT = 17152;
constexpr int L_Q = 20224;
constexpr int L_SINK = 18688;
constexpr int LDS_BYTES = 22 * 1024;
constexpr int NE = 18, WE = 8, WE0 = 5, WK = 8, WV = 8, WL = 10, TS0 = 8;
constexpr int TE = 109, TK = 113, TV2 = 113, ERG = 60, TL = 123, TOL = 50;
constexpr int XL = 440, BOFF = 60, TB = 930, EOFF = 130;
constexpr int TV = 18 * TE + 16 * TK + ERG + 16 * TV2;
constexpr int TQ = 1550, LOFF = 150, OLOFF = 200, OOFF = 870, FOFF = 1820;
constexpr int RT = 32, RK = 16, RW = 4;

__device__ __forceinline__ rsrc_t mk_rsrc(const void* p, u32 bytes) { return __builtin_amdgcn_make_buffer_rsrc((void*)p, 0, bytes, 0x00020000); }
__device__ __forceinline__ v4u ld16(rsrc_t r, int v, int s) { return __builtin_amdgcn_raw_buffer_load_b128(r, v, s, AUX_NT); }
__device__ __forceinline__ v4u ld16c(rsrc_t r, int v, int s) { return __builtin_amdgcn_raw_buffer_load_b128(r, v, s, AUX_SC1); }
__device__ __forceinline__ void st16c(v4u x, rsrc_t r, int v, int s) { __builtin_amdgcn_raw_buffer_store_b128(x, r, v + s, 0, AUX_SC1); }
__device__ __forceinline__ void st8c(v2u x, rsrc_t r, int v, int s) { __builtin_amdgcn_raw_buffer_store_b64(x, r, v, s, AUX_SC1); }
__device__ __forceinline__ float asf(u32 x) { return __builtin_bit_cast(float, x); }
__device__ __forceinline__ u32 asu(float x) { return __builtin_bit_cast(u32, x); }
__device__ __forceinline__ u32 pkbf(float lo, float hi) { bf2 r = {(__bf16)lo, (__bf16)hi}; return __builtin_bit_cast(u32, r); }
__device__ __forceinline__ float rbf(float x) { return asf(pkbf(x, x) << 16); }
template <int C> __device__ __forceinline__ u32 dppu(u32 x) { return (u32)__builtin_amdgcn_mov_dpp((int)x, C, 0xf, 0xf, true); }
template <int C> __device__ __forceinline__ float dppf(float x) { return asf(dppu<C>(asu(x))); }
constexpr int XOR1 = 0xB1, XOR2 = 0x4E, HMIR = 0x141, ROR8 = 0x128, ROR4 = 0x124;
__device__ __forceinline__ float x16_sum(float x) { auto s = __builtin_amdgcn_permlane16_swap(asu(x), asu(x), false, false); return asf(s[0]) + asf(s[1]); }
__device__ __forceinline__ float x32_sum(float x) { auto s = __builtin_amdgcn_permlane32_swap(asu(x), asu(x), false, false); return asf(s[0]) + asf(s[1]); }
__device__ __forceinline__ float x16_max(float x) { auto s = __builtin_amdgcn_permlane16_swap(asu(x), asu(x), false, false); return fmaxf(asf(s[0]), asf(s[1])); }
__device__ __forceinline__ float x32_max(float x) { auto s = __builtin_amdgcn_permlane32_swap(asu(x), asu(x), false, false); return fmaxf(asf(s[0]), asf(s[1])); }
__device__ __forceinline__ float sum8(float x) { x += dppf<XOR1>(x); x += dppf<XOR2>(x); x += dppf<HMIR>(x); return x; }
__device__ __forceinline__ u32 or8(u32 x) { x |= dppu<XOR1>(x); x |= dppu<XOR2>(x); x |= dppu<HMIR>(x); return x; }
__device__ __forceinline__ float max8(float x) { x = fmaxf(x, dppf<XOR1>(x)); x = fmaxf(x, dppf<XOR2>(x)); x = fmaxf(x, dppf<HMIR>(x)); return x; }
__device__ __forceinline__ float sum64(float x) { x = sum8(x); x += dppf<ROR8>(x); return x32_sum(x16_sum(x)); }
__device__ __forceinline__ float dot2(u32 a, u32 b, float c) { return __builtin_amdgcn_fdot2_f32_bf16(__builtin_bit_cast(bf2, a), __builtin_bit_cast(bf2, b), c, false); }
__device__ __forceinline__ f4 mfma(v4u a, v4u b, f4 c) { return __builtin_amdgcn_mfma_f32_16x16x32_bf16(__builtin_bit_cast(bf8, a), __builtin_bit_cast(bf8, b), c, 0, 0, 0); }
typedef __bf16 bf16v __attribute__((ext_vector_type(16)));
__device__ __forceinline__ f4 smfma(v4u a, v4u b0, v4u b1, f4 c, u32 idx) {
  return __builtin_amdgcn_smfmac_f32_16x16x64_bf16(__builtin_bit_cast(bf8, a), __builtin_bit_cast(bf16v, __builtin_shufflevector(b0, b1, 0, 1, 2, 3, 4, 5, 6, 7)), c, idx, 0, 0); }
template <int T> __device__ __forceinline__ v4u onehot(u32 p, u32 pm, const v4u& mask) {
  constexpr int a = (T & 3) * 0x55, b = (3 - (T & 3)) * 0x55;
  v4u r;
  if (T < 4) { r.x = dppu<a>(p) & mask.x; r.y = dppu<a>(p) & mask.y; r.z = dppu<b>(pm) & mask.z; r.w = dppu<b>(pm) & mask.w; }
  else { r.x = dppu<b>(pm) & mask.x; r.y = dppu<b>(pm) & mask.y; r.z = dppu<a>(p) & mask.z; r.w = dppu<a>(p) & mask.w; }
  return r;
}
__device__ __forceinline__ u32 tag1(float a, u32 t8, u32 sel = 0x07060500u) { return __builtin_amdgcn_perm(asu(a), t8, sel); }
__device__ __forceinline__ v4u tagged(f4 a, u32 t8, u32 t8b, u32 sel) { return v4u{tag1(a.x, t8, sel), asu(a.y), tag1(a.z, t8b, sel), asu(a.w)}; }
__device__ __forceinline__ float red4(f4 v) {
  auto p = __builtin_amdgcn_permlane32_swap(asu(v.x), asu(v.y), false, false), q = __builtin_amdgcn_permlane32_swap(asu(v.z), asu(v.w), false, false);
  auto u = __builtin_amdgcn_permlane16_swap(asu(asf(p[0]) + asf(p[1])), asu(asf(q[0]) + asf(q[1])), false, false);
  return asf(u[0]) + asf(u[1]);
}
#define RETRY(b, d, o, rs, so) "v_cmp_ne_u32 vcc, 0, " b "\n\ts_mov_b64 exec, vcc\n\tbuffer_load_dwordx4 " d ", " o ", " rs ", " so " offen sc1\n\ts_mov_b64 exec, -1\n\t"
#define FENCE() asm volatile("" ::: "memory")
#define SB() __builtin_amdgcn_sched_barrier(0)
#define VMW(n) asm volatile("s_waitcnt vmcnt(%0)" :: "i"(n) : "memory")
#define CLK_READ "s_memrealtime vcc\n\ts_waitcnt lgkmcnt(0)\n\ts_lshl_b32 vcc_lo, vcc_lo, 3\n\ts_sub_u32 vcc_lo, vcc_lo, %0\n\t"
#define DPACE(t, W, tau) do { if ((t) >= (W)) VMW((W) - 1); asm volatile("s_add_u32 %0, %0, %1\n\ts_branch 2f\n1:\n\ts_sleep 1\n2:\n\t" CLK_READ "s_cmp_lt_i32 vcc_lo, -16\n\ts_cbranch_scc1 1b" : "+s"(dl) : "s"(tau) : "memory", "scc", "vcc"); } while (0)
#define WAITT(when) asm volatile("s_branch 2f\n1:\n\ts_sleep 6\n2:\n\t" CLK_READ "s_cmpk_lt_i32 vcc_lo, -200\n\ts_cbranch_scc1 1b\n\ts_cmp_lt_i32 vcc_lo, 0\n\ts_cbranch_scc1 2b" :: "s"(when) : "memory", "scc", "vcc")
#define GETANCHOR() ((u32)__builtin_amdgcn_readfirstlane(*(volatile LAS u32*)(lds + L_M + 68)))
#define WAITFLAG(i) do { _Pragma("nounroll") for (int sp = 0; sp < SPIN_CAP && __builtin_amdgcn_readfirstlane(cnt[320 + (i)]) == 0u; ++sp) __builtin_amdgcn_s_sleep(1); FENCE(); } while (0)
#define SETFLAG(i) do { FENCE(); cnt[320 + (i)] = 1u; FENCE(); } while (0)
#define GETTAG() ((u32)__builtin_amdgcn_readfirstlane(*(LAS u32*)(lds + L_M + 64)))
#define ARRIVE(row) __hip_atomic_fetch_add((LAS u32*)(lds + L_CNT + (row) * 256 + lane * 4), 1u, __ATOMIC_RELAXED, __HIP_MEMORY_SCOPE_WORKGROUP)
#define BAR0() asm volatile("s_barrier" ::: "memory")
template <int I, int N, class F> __device__ __forceinline__ void sfor(F&& f) { if constexpr (I < N) { f(std::integral_constant<int, I>{}); sfor<I + 1, N>(f); } }

struct Slot { const char* base; u32 nrec, gmulA, gmulB, wmul, stepA, stepB; };
struct Args { Slot slot[32]; const char *x, *kc, *vc, *w2; char *out, *ws; };
static_assert(sizeof(Slot) == 32 && sizeof(Args) == 1072, "no padding");
typedef u32 v8u __attribute__((ext_vector_type(8)));

__global__ void __launch_bounds__(768, 3) fused_decode(Args args_) {
  extern __shared__ __attribute__((aligned(16))) char lds_[];
  LAS char* lds = (LAS char*)lds_;
  volatile LAS u32* cnt = (volatile LAS u32*)(lds + L_CNT);
  const int b = blockIdx.x;
  const CAS char* ka = (const CAS char*)__builtin_amdgcn_kernarg_segment_ptr();
  v8u sl = *(const CAS v8u*)(ka + (u32)((b >> 3) << 5));
  v8u pa = *(const CAS v8u*)(ka + 1024);
  v4u pb = *(const CAS v4u*)(ka + 1056);
  SB();
  const int tid = threadIdx.x, lane = tid & 63;
  const int wave = __builtin_amdgcn_readfirstlane(tid >> 6), w = wave & 3;
  const int s = b >> 4, gh = (b >> 3) & 1, xp = (b & 7) ^ max(s - 12, 1);
  const int g = b & 15, gp = xp + 8 * gh;
  const int ga_ = xp >> 1, gb_ = xp & 1;
  const int r = lane >> 3, c8 = lane & 7;
  const int e4 = s - 4;
  const bool isq = s < 4, iso = s >= 13;
  const int mo = (12 - s) >> 31, ro = ((r * 40) & 48) ^ ((r >> 2) * 34);
  const int i4 = lane >> 4, c16 = lane & 15;
  int voff = (i4 ^ ((i4 ^ ro) & mo)) * (9216 - (1024 & (e4 >> 31)) - (8064 & mo)) + (lane & (15 ^ (8 & mo))) * 16;
  u64 pc_; u32 poff = (((b >> 3) << 3) + wave - 4) << 7, junk, junk2, epoch, xb; v4u xa;
  int xo16 = lane * 16, xo2 = lane * 2;
  asm volatile("" : "+v"(voff), "+v"(xo16), "+v"(xo2) : "s"(poff), "s"(w), "s"(ga_), "s"(gb_));
  SB();
  const u64 xptr = pa[0] | ((u64)pa[1] << 32), wptr = pb[2] | ((u64)pb[3] << 32);
  const int sbase = ga_ * sl[3] + gb_ * sl[4] + w * sl[5], stepA = sl[6], stepB = sl[7];
  const u64 mptr = sl[0] | ((u64)sl[1] << 32);
  u32 xj;
  asm volatile("s_cmp_lt_u32 %7, 4\n\ts_cbranch_scc1 2f\n\ts_cmp_lg_u32 %7, 8\n\ts_cbranch_scc1 3f\n\tv_mov_b32 %5, %13\n\ts_load_dword %2, %12, 0x0\n\t"
               "global_load_dwordx4 %3, %9, %11\n\tglobal_load_ushort %4, %10, %11 offset:1024\n\tglobal_load_dword %5, %5, %14 nt\n"
               "3:\n\ts_getpc_b64 %0\n1:\n\ts_min_u32 %1, %8, .Lfd_code_end-1b-64\n\ts_load_dword %6, %0, %1 offset:64\n\ts_load_dword %1, %0, %1\n2:"
               : "=&s"(pc_), "=&s"(junk), "=&s"(epoch), "=&v"(xa), "=&v"(xb), "=&v"(xj), "=&s"(junk2)
               : "s"(wave), "s"(poff), "v"(xo16), "v"(xo2), "s"(xptr), "s"(wptr), "s"(sbase), "s"(mptr) : "memory", "scc");
  SB();
  const char* mat = (const char*)(sl[0] | ((u64)sl[1] << 32));
  const u32 nrec = sl[2];
#define PTR(v, i) ((char*)((v)[i] | ((u64)(v)[(i) + 1] << 32)))
  const char *x = PTR(pa, 0), *kc = PTR(pa, 2), *vc = PTR(pa, 4), *w2 = PTR(pa, 6);
  char *out = PTR(pb, 0), *ws = PTR(pb, 2);
#define MKMASK() v4u mask; { const u32 hm_ = (lane & 1) ? 0xFFFF0000u : 0x0000FFFFu; const int q_ = c8 >> 1; \
    mask.x = q_ == 0 ? hm_ : 0u; mask.y = q_ == 1 ? hm_ : 0u; mask.z = q_ == 2 ? hm_ : 0u; mask.w = q_ == 3 ? hm_ : 0u; }

  if (__builtin_expect(wave < 4, 1)) {
    const rsrc_t re = mk_rsrc(mat, nrec);
    v4u e[24];
    int so_ = sbase;
    e[0] = ld16(re, voff, so_);
    SB();
    u32 dl = (u32)__builtin_amdgcn_s_memrealtime() * 8;
    const u32 taue = TE;
    *(volatile LAS u32*)(lds + L_M + 72) = dl - 0x40000000u;
#define ELOAD(t) do { so_ += (t) % 3 ? stepB : stepA; if ((t) == 12) so_ -= (15 * 1152) & mo; if ((t) == WE0) { VMW((t) - 1); const u32 n_ = (u32)__builtin_amdgcn_s_memrealtime() * 8 - taue; dl = (int)(n_ - dl) > 0 ? n_ : dl; } u32 a_; if ((t) >= TS0 && (t) < 17) a_ = *(volatile LAS u32*)(lds + L_M + 72); DPACE(t, WE, taue); e[t] = ld16(re, voff, so_); \
      if ((t) >= TS0 && (t) < 17) { SB(); const u32 n_ = __builtin_amdgcn_readfirstlane(a_) + (t) * taue; dl = n_ - dl + 1600u < 3200u ? n_ : dl; } } while (0)
    sfor<1, NE>([&](auto T) { constexpr int t = T.value; ELOAD(t); });
    const rsrc_t rw = mk_rsrc(ws, WS_END);
    const v4u rd4 = {pb[2], pb[3] & 0xFFFFu, (u32)WS_END, 0x00020000u};
    MKMASK();
    const bool dvalid = (lane >> 5) == (r & 1);
    const int dcol = (c8 * 8 + ((lane >> 4) & 1) * 4) * 4, dpart = 2 * w + (lane >> 5);
#define PUBLISH8() do { SB(); u32 sel = 0x07060500u; so = old2 == 3 ? so : OOB; asm volatile("" : "+v"(so), "+v"(sel)); SB(); \
      st16c(tagged((((((z[0] + z[1]) + (z[2] + z[3])) + z[4]) + z[5]) + z[6]) + z[7], t8, t8b, sel), rw, so, 0); } while (0)
#define FETCH_Q() if (w == 1) { WAITT(anchor + 18 * TE + TQ); v4u qv[4]; \
      _Pragma("nounroll") for (int sp = 0; sp < SPIN_CAP; ++sp) { FENCE(); u32 bad = 0; \
        _Pragma("unroll") for (int m = 0; m < 4; ++m) qv[m] = ld16c(rw, lane * 16, qbox_off(g, m)); \
        _Pragma("unroll") for (int m = 0; m < 4; ++m) bad |= (qv[m].y ^ tag) | (qv[m].w ^ tag); \
        if (__all(bad == 0u)) break; } \
      *(LAS u32*)(lds + L_Q + lane * 4) = pkbf(asf(qv[0].x) + asf(qv[1].x), asf(qv[0].z) + asf(qv[1].z)); \
      *(LAS u32*)(lds + L_Q + 256 + lane * 4) = pkbf(asf(qv[2].x) + asf(qv[3].x), asf(qv[2].z) + asf(qv[3].z)); \
      SETFLAG(0); }
    if (!iso) {
      sfor<NE, 18>([&](auto T) { constexpr int t = T.value; ELOAD(t); });
      SB();
      BAR0();
      const u32 tag = GETTAG(), t8 = tag & 255u, t8b = t8 ^ ((tag >> 8) & 254u) ^ 1u, anchor = GETANCHOR();
      const int kh = s == 12 ? gb_ : (s & 1);
      u32 xv[2];
#pragma unroll
      for (int u = 0; u < 2; ++u) { const int tt = 16 * u + (u ? c16 & 1 : c16); xv[u] = *(LAS u32*)(lds + L_XN + (RT * tt + RK * kh + RW * w + i4) * 4); }
      f4 acc = {0.f, 0.f, 0.f, 0.f};
      sfor<0, 18>([&](auto T) { constexpr int t = T.value; const u32 xb_ = dppu<0x150 + (t & 15)>(xv[t >> 4]); acc = mfma(v4u{xb_, xb_, xb_, xb_} & mask, e[t], acc); });
      *(LAS f4*)(lds + L_RED + w * 512 + c16 * 32 + (i4 & 1) * 16) = acc;
      FENCE();
      const u32 old = ARRIVE(0);
      FENCE();
      f2 y[4];
#pragma unroll
      for (int k = 0; k < 4; ++k) y[k] = *(LAS f2*)(lds + L_RED + k * 512 + lane * 8);
      SB();
      const f2 a = (y[0] + y[1]) + (y[2] + y[3]);
      if (isq) st16c(v4u{asu(a.x), tag, asu(a.y), tag}, rw, old == 3 ? lane * 16 : OOB, qbox_off(gp, s));
      else st8c(v2u{tag1(a.x, t8), tag1(a.y, t8b)}, rw, old == 3 ? lane * 8 : OOB,
                hbox_off(s == 12 ? 4 * (32 + ga_) + 2 * gh + gb_ : 4 * (2 * gp + ((s >> 1) & 1)) + 2 * (e4 >> 2) + (s & 1)));
      FETCH_Q();
      const int qi = b;
      if (!isq || qi < 18) {
        const int rho = isq ? 16 + (qi >= 9) : g, cb = isq ? (qi >= 9 ? qi - 9 : qi) : e4;
        const int hoff = i4 * RS + c16 * 16 + (w & 1) * 256, hs = hbox_off(4 * (2 * rho + (w >> 1)));
        const rsrc_t rl = mk_rsrc(w2, FF * 1152);
        const int rb = ((r * 33) & 34) | (r >> 2);
        const int lo = rb * 1152 + c8 * 16, ls = (256 * rho + 64 * w) * 1152 + cb * 128;
        WAITT(anchor + TV);
        v4u hg = ld16c(rw, hoff, hs);
        WAITFLAG(8 + w);
        v4u lw[8];
        const u32 tl_ = TL; dl = anchor + (TV + LOFF - TL);
        sfor<0, 8>([&](auto T) { constexpr int t = T.value; DPACE(t, WL, tl_); lw[t] = ld16(rl, lo, ls + t * 4608); });
        SB();
        hg ^= v4u{t8, t8b, t8, t8b};
        if (!__all(((hg.x | hg.y | hg.z | hg.w) & 255u) == 0u)) {
          _Pragma("nounroll") for (int sp = 0; sp < SPIN_CAP; ++sp) { FENCE(); hg = ld16c(rw, hoff, hs) ^ v4u{t8, t8b, t8, t8b}; if (__all(((hg.x | hg.y | hg.z | hg.w) & 255u) == 0u)) break; }
        }
        const auto ha = __builtin_amdgcn_permlane16_swap(hg.x, hg.z, false, false), hb_ = __builtin_amdgcn_permlane16_swap(hg.y, hg.w, false, false);
        const auto hc = __builtin_amdgcn_permlane32_swap(asu(asf(ha[0]) + asf(ha[1])), asu(asf(hb_[0]) + asf(hb_[1])), false, false);
        const float gt = rbf(asf(hc[0])), uv = rbf(asf(hc[1]));
        const float hv = rbf(gt * __builtin_amdgcn_rcpf(1.f + __expf(-gt))) * uv;
        const u32 hp = pkbf(hv, hv), hm = dppu<HMIR>(hp);
        v4u A[8];
        sfor<0, 8>([&](auto T) { constexpr int t = T.value; A[t] = onehot<t>(hp, hm, mask); });
        int wr = dvalid ? L_RED2 + dpart * 256 + dcol : L_SINK + lane * 16, rd = L_RED2 + (lane & 15) * 16, ca = L_CNT + 1024 + lane * 4;
        int so = lane < 16 ? WS_FBOX + (rho & 15) * RS + (1 + (rho >> 4)) * 4096 + cb * 256 + lane * 16 : OOB;
        asm volatile("" : "+v"(A[0]), "+v"(A[1]), "+v"(A[2]), "+v"(A[3]), "+v"(A[4]), "+v"(A[5]), "+v"(A[6]), "+v"(A[7]), "+v"(wr), "+v"(rd), "+v"(ca), "+v"(so));
        f4 a2 = {0.f, 0.f, 0.f, 0.f};
#pragma unroll
        for (int t = 0; t < 8; ++t) a2 = mfma(A[t], lw[t], a2);
        *(LAS f4*)(lds + wr) = a2;
        FENCE();
        const u32 old2 = __hip_atomic_fetch_add((LAS u32*)(lds + ca), 1u, __ATOMIC_RELAXED, __HIP_MEMORY_SCOPE_WORKGROUP);
        FENCE();
        f4 z[8];
#pragma unroll
        for (int k = 0; k < 8; ++k) z[k] = *(LAS f4*)(lds + rd + k * 256);
        PUBLISH8();
      } else if (qi >= 46 && w == 0) {
        const int kap = qi - 46;
        const int col = kap * 32 + c8 * 4 + ((lane >> 4) & 1) * 2 + (lane >> 5);
        float xr = asf((u32)*(const GAS unsigned short*)(x + col * 2) << 16);
        { u32 j_; asm volatile("s_load_dword %0, %1, 0x0\n\ts_waitcnt lgkmcnt(0)" : "=&s"(j_) : "s"(out) : "memory"); }
        const rsrc_t ro = mk_rsrc(out, DM * 2);
        int oo = (lane & 8) ? OOB : col * 2;
        u32 m16 = lane < 16 ? t8 : 0u, m16b = lane < 16 ? t8b : 0u;
        asm volatile("" : "+v"(xr), "+v"(oo), "+v"(m16), "+v"(m16b));
        const int fs = WS_FBOX + kap * 128, fo = r * RS + c8 * 16, fo2 = lane < 16 ? fo : OOB;
        const v4u tv = {t8, 0u, t8b, 0u}, tv2 = {m16, 0u, m16b, 0u};
        auto red = [&](v4u a, v4u c) { const float y = red4(__builtin_bit_cast(f4, a) + __builtin_bit_cast(f4, c)); return y + dppf<ROR8>(y); };
        auto orv = [](v4u a) { return a.x | a.z; };
        WAITT(anchor + (TV + FOFF));
        v4u f0 = ld16c(rw, fo, fs + 4096), f1 = ld16c(rw, fo, fs + 4096 + 8 * RS), f2_ = ld16c(rw, fo2, fs + 8192);
        v4u s0 = ld16c(rw, fo, fs), s1 = ld16c(rw, fo, fs + 8 * RS);
        SB();
        f0 ^= tv; f1 ^= tv; f2_ ^= tv2;
        if (__builtin_expect(!__all(((orv(f0) | orv(f1) | orv(f2_)) & 255u) == 0u), 0)) {
          _Pragma("nounroll") for (int sp = 0; sp < SPIN_CAP; ++sp) {
            FENCE();
            f0 = ld16c(rw, fo, fs + 4096) ^ tv; f1 = ld16c(rw, fo, fs + 4096 + 8 * RS) ^ tv; f2_ = ld16c(rw, fo2, fs + 8192) ^ tv2;
            if (__all(((orv(f0) | orv(f1) | orv(f2_)) & 255u) == 0u)) break;
          }
        }
        f1 = __builtin_bit_cast(v4u, __builtin_bit_cast(f4, f1) + __builtin_bit_cast(f4, f2_));
        s0 ^= tv; s1 ^= tv;
        const float fv = rbf(red(f0, f1));
        float sv = rbf(red(s0, s1)), gg = rbf(fv * __builtin_amdgcn_rcpf(1.f + __expf(-fv)));
        u32 bads = (orv(s0) | orv(s1)) & 255u;
        asm volatile("" : "+v"(gg), "+v"(sv), "+v"(bads));
        if (__builtin_expect(!__all(bads == 0u), 0)) {
          _Pragma("nounroll") for (int sp = 0; sp < SPIN_CAP; ++sp) {
            FENCE();
            const u32 any = or8((orv(s0) & 255u) | (orv(s1) << 24));
            u32 b0 = any & 255u, b1 = any >> 24;
            s0 ^= tv; s1 ^= tv;
            asm volatile(RETRY("%2", "%0", "%4", "%5", "%6") RETRY("%3", "%1", "%4", "%5", "%7") "s_waitcnt vmcnt(0)" : "+v"(s0), "+v"(s1) : "v"(b0), "v"(b1), "v"(fo), "s"(rd4), "s"(fs), "s"(fs + 8 * RS) : "memory", "vcc");
            s0 ^= tv; s1 ^= tv;
            if (__all(((orv(s0) | orv(s1)) & 255u) == 0u)) break;
          }
          sv = rbf(red(s0, s1));
          asm volatile(".Lfd_code_end:" : "+v"(sv));
        }
        const float o = xr + rbf(sv * gg);
        __builtin_amdgcn_raw_buffer_store_b16((unsigned short)pkbf(o, o), ro, oo, 0, AUX_SC1 | 1);
        if (kap == 0 && lane == 0) __hip_atomic_store((GAS u32*)ws, tag, __ATOMIC_RELAXED, __HIP_MEMORY_SCOPE_AGENT);
      }
    } else {
      SB();
      BAR0();
      const u32 tag = GETTAG(), t8 = tag & 255u, t8b = t8 ^ ((tag >> 8) & 254u) ^ 1u, anchor = GETANCHOR();
      FETCH_Q();
      WAITFLAG(8 + w);
      const u32 tl_ = TOL; dl = anchor + (TV + OLOFF - TOL);
      sfor<NE, 24>([&](auto T) { constexpr int t = T.value; DPACE(t - NE, WL, tl_); e[t] = ld16(re, voff + (t % 3) * 128, sbase + (4 * ((t / 3) & 3) + (t / 12)) * 1152); });
      SB();
      int oo = i4 * 4 * RS + c16 * 16, wr = L_RED2 + dpart * 768 + dcol, rd = L_RED2 + lane * 16, ca = L_CNT + 1024 + lane * 4;
      int so = lane < 48 ? WS_FBOX + gp * RS + (s - 13) * 768 + lane * 16 : OOB;
      int mlo = ((4 * i4 + c16) & 15) * RS;
      wr = dvalid ? wr : L_SINK + lane * 16;
      u32 mA = c8 < 4 ? 0xFFFFu : 0u, mB = mA ^ 0xFFFFu, idx = (lane & 3) * 0x5555;
      f4 a0 = {0.f, 0.f, 0.f, 0.f}, a1 = a0, a2 = a0;
      asm volatile("" : "+v"(mA), "+v"(mB), "+v"(idx), "+v"(a0), "+v"(a1), "+v"(a2));
      asm volatile("" : "+v"(mlo), "+v"(oo), "+v"(wr), "+v"(rd), "+v"(so), "+v"(ca));
      WAITT(anchor + (TV + OOFF));
      v4u ov[4];
      float ex, lv;
      u32 bad, badm;
      const int os = WS_OBOX + gp * 1024 + w * 256, mls = WS_ML + gp * 128 + (w >> 1) * 16;
      v4u ml = ld16c(rw, mlo, mls);
#pragma unroll
      for (int u = 0; u < 4; ++u) ov[u] = ld16c(rw, oo, os + u * RS);
      SB();
#define DM_ " row_mask:0xf bank_mask:0xf\n\t"
#define MERGE() do {         u32 mx, x0 = ov[0].x, z0 = ov[0].z, x1 = ov[1].x, z1 = ov[1].z, x2 = ov[2].x, z2 = ov[2].z, x3 = ov[3].x, z3 = ov[3].z; \
        asm volatile("v_max_f32_dpp %0, %3, %3 row_ror:8" DM_ "v_bitop3_b32 %1, %4, %5, %6 bitop3:0x7e\n\tv_xor_b32 %2, %7, %2" : "=&v"(mx), "=&v"(badm), "+v"(x0) : "v"(ml.x), "v"(ml.y), "v"(ml.w), "s"(tag), "s"(t8)); \
        asm volatile("v_max_f32_dpp %0, %0, %0 row_ror:4" DM_ "v_xor_b32 %1, %4, %1\n\tv_xor_b32 %2, %3, %2" : "+v"(mx), "+v"(z0), "+v"(x1) : "s"(t8), "s"(t8b)); \
        asm volatile("v_max_f32_dpp %0, %0, %0 row_ror:2" DM_ "v_xor_b32 %1, %4, %1\n\tv_xor_b32 %2, %3, %2" : "+v"(mx), "+v"(z1), "+v"(x2) : "s"(t8), "s"(t8b)); \
        asm volatile("v_max_f32_dpp %0, %0, %0 row_ror:1" DM_ "v_xor_b32 %1, %6, %1\n\tv_sub_f32 %0, %5, %0\n\tv_mul_f32 %0, 0x3fb8aa3b, %0\n\tv_exp_f32 %0, %0\n\tv_xor_b32 %2, %4, %2\n\tv_xor_b32 %3, %6, %3" \
                     : "+v"(mx), "+v"(z2), "+v"(x3), "+v"(z3) : "s"(t8), "v"(ml.x), "s"(t8b)); \
        ex = asf(mx); lv = asf(ml.z); \
        ov[0].x = x0; ov[0].z = z0; ov[1].x = x1; ov[1].z = z1; ov[2].x = x2; ov[2].z = z2; ov[3].x = x3; ov[3].z = z3; \
        bad = badm | (((x0 | z0 | x1) | (z1 | x2 | z2) | (x3 | z3)) & 255u); } while (0)
      MERGE();
      if (__builtin_expect(!__all(bad == 0u), 0)) {
        _Pragma("nounroll") for (int sp = 0; sp < SPIN_CAP; ++sp) {
          u32 bu[4];
#pragma unroll
          for (int u = 0; u < 4; ++u) { bu[u] = (ov[u].x | ov[u].z) & 255u; ov[u].x ^= t8; ov[u].z ^= t8b; }
          u32 any = bu[0] | (bu[1] << 8) | (bu[2] << 16) | (bu[3] << 24);
          any = or8(any); any |= dppu<ROR8>(any);
#pragma unroll
          for (int u = 0; u < 4; ++u) bu[u] = any & (255u << (8 * u));
          asm volatile(RETRY("%5", "%0", "%10", "%12", "%13") RETRY("%6", "%1", "%11", "%12", "%14") RETRY("%7", "%2", "%11", "%12", "%15") RETRY("%8", "%3", "%11", "%12", "%16") RETRY("%9", "%4", "%11", "%12", "%17") "s_waitcnt vmcnt(0)\n\ts_nop 3"
                       : "+v"(ml), "+v"(ov[0]), "+v"(ov[1]), "+v"(ov[2]), "+v"(ov[3])
                       : "v"(badm), "v"(bu[0]), "v"(bu[1]), "v"(bu[2]), "v"(bu[3]), "v"(mlo), "v"(oo), "s"(rd4), "s"(mls), "s"(os), "s"(os + RS), "s"(os + 2 * RS), "s"(os + 3 * RS) : "memory", "vcc");
          MERGE();
          if (__all(bad == 0u)) break;
        }
      }
      float lsum = ex * lv;
      asm volatile("" : "+v"(lsum));
      const float w0 = dppf<0x150>(ex), w1 = dppf<0x151>(ex); SB();
      lsum += dppf<ROR8>(lsum); f4 o4 = __builtin_bit_cast(f4, ov[0]) * w0; SB();
      lsum += dppf<ROR4>(lsum); o4 += __builtin_bit_cast(f4, ov[1]) * w1; SB();
      lsum += dppf<XOR2>(lsum); o4 += __builtin_bit_cast(f4, ov[2]) * dppf<0x152>(ex); SB();
      lsum += dppf<XOR1>(lsum); o4 += __builtin_bit_cast(f4, ov[3]) * dppf<0x153>(ex); SB();
      const float of = red4(o4) * __builtin_amdgcn_rcpf(lsum);
      const u32 op = pkbf(of, of), om = dppu<HMIR>(op);
#define SPA(t) v4u{dppu<(t) * 0x55>(op) & mA, dppu<(3 - (t)) * 0x55>(om) & mB, dppu<(3 - (t)) * 0x55>(om) & mA, dppu<(t) * 0x55>(op) & mB}
      v4u A = SPA(0), An;
      SB();
      sfor<0, 4>([&](auto T) { constexpr int t = T.value;
        if constexpr (t < 3) An = SPA(t + 1);
        a0 = smfma(A, e[3 * t], e[3 * t + 12], a0, idx); a1 = smfma(A, e[3 * t + 1], e[3 * t + 13], a1, idx); a2 = smfma(A, e[3 * t + 2], e[3 * t + 14], a2, idx);
        if constexpr (t < 3) { A = An;
          __builtin_amdgcn_sched_group_barrier(0x8, 1, 0); __builtin_amdgcn_sched_group_barrier(0x2, 2, 0); __builtin_amdgcn_sched_group_barrier(0x8, 1, 0); __builtin_amdgcn_sched_group_barrier(0x2, 2, 0);
          __builtin_amdgcn_sched_group_barrier(0x8, 1, 0); SB(); } });
      *(LAS f4*)(lds + wr) = a0; *(LAS f4*)(lds + wr + 256) = a1; *(LAS f4*)(lds + wr + 512) = a2;
      FENCE();
      const u32 old2 = __hip_atomic_fetch_add((LAS u32*)(lds + ca), 1u, __ATOMIC_RELAXED, __HIP_MEMORY_SCOPE_WORKGROUP);
      FENCE();
      f4 z[8];
#pragma unroll
      for (int k = 0; k < 8; ++k) z[k] = *(LAS f4*)(lds + rd + k * 768);
      PUBLISH8();
    }
  } else {
    const rsrc_t rw = mk_rsrc(ws, WS_END);
    const size_t hb = ((size_t)g * SK + (SK - WIN)) * 256;
    const int vo = lane * 16;
    const int kb = w * 1024, ra = g + s;
#define KOFF(t) (kb + ((((t) + ra) & 15) << 12) + ((t) << 16))
    const u32 tau = TK, k0 = 18 * TE - tau;
    if (wave < 8) {
      const rsrc_t rk = mk_rsrc(kc + hb, WIN * 256);
      if (w == 0) { *(LAS v4u*)(lds + L_CNT + lane * 16) = v4u{0u, 0u, 0u, 0u}; *(LAS v2u*)(lds + L_CNT + 1024 + lane * 8) = v2u{0u, 0u}; }
      {
        const char* pp_ = w < 2 ? (w ? vc : kc) + hb + s * 65536 : w2 + (s < 4 ? 16 + (s & 1) : g) * (256 * 1152) + (w - 2) * (255 * 1152) + (s & 7) * 128;
        u32 j_;
        asm volatile("s_waitcnt lgkmcnt(0)\n\ts_load_dword %0, %1, 0x0\n\ts_waitcnt lgkmcnt(0)" : "=&s"(j_) : "s"(pp_), "s"(junk), "s"(junk2) : "memory");
      }
      asm volatile("s_waitcnt lgkmcnt(0)\n\ts_barrier" :: "s"(junk), "s"(junk2) : "memory");
      u32 dl = GETANCHOR() + k0;
      v4u K[16], q[2];
      sfor<0, 16>([&](auto T) { constexpr int t = T.value; DPACE(t, WK, tau); K[t] = ld16(rk, vo, KOFF(t)); });
      SETFLAG(4 + w);
      __builtin_amdgcn_s_setprio(2);
      WAITFLAG(0);
      q[0] = *(LAS v4u*)(lds + L_Q + (lane & 15) * 16); q[1] = *(LAS v4u*)(lds + L_Q + 256 + (lane & 15) * 16);
    float sc[2];
#pragma unroll
    for (int hf = 0; hf < 2; ++hf) {
      float y[16];
#pragma unroll
      for (int t = 0; t < 8; ++t)
#pragma unroll
        for (int h = 0; h < 2; ++h) { const v4u kk = K[8 * hf + t];
          y[8 * h + t] = dot2(kk.w, q[h].w, dot2(kk.z, q[h].z, dot2(kk.y, q[h].y, dot2(kk.x, q[h].x, 0.f)))); }
#define TRS(H, C) _Pragma("unroll") for (int j = 0; j < (H); ++j) { const float lo_ = y[j] + dppf<C>(y[j]), hi_ = y[j + (H)] + dppf<C>(y[j + (H)]); y[j] = (lane & (H)) ? hi_ : lo_; }
      TRS(8, ROR8) TRS(4, HMIR) TRS(2, XOR2) TRS(1, XOR1)
#undef TRS
      sc[hf] = rbf(rbf(y[0]) * 0.08838834764831845f);
    }
      const int h = r & 1;
      float mx = x32_max(x16_max(max8(fmaxf(sc[0], sc[1]))));
      *(volatile LAS float*)(lds + L_M + h * 16 + w * 4) = mx;
      FENCE();
      u32 na = __builtin_amdgcn_readfirstlane(ARRIVE(1)) + 1;
      _Pragma("nounroll") for (int sp = 0; sp < SPIN_CAP && na != 4u; ++sp) na = __builtin_amdgcn_readfirstlane(cnt[64]);
      FENCE();
      { const f4 mm = *(LAS f4*)(lds + L_M + h * 16); mx = fmaxf(fmaxf(mm.x, mm.y), fmaxf(mm.z, mm.w)); }
      const float p0 = __builtin_amdgcn_exp2f((sc[0] - mx) * 1.4426950408889634f), p1 = __builtin_amdgcn_exp2f((sc[1] - mx) * 1.4426950408889634f);
      *(LAS v2u*)(lds + L_P + w * 512 + lane * 8) = v2u{pkbf(p0, p0), pkbf(p1, p1)};
      *(volatile LAS float*)(lds + L_M + 32 + h * 16 + w * 4) = x32_sum(x16_sum(sum8(p0 + p1)));
      SETFLAG(12 + w);
    } else {
      u32 tg, c0;
      if (w == 0) {
        asm volatile("s_waitcnt vmcnt(0)" : "+v"(xa), "+v"(xb), "+v"(xj));
        asm volatile("s_memrealtime vcc\n\ts_waitcnt lgkmcnt(0)\n\ts_mul_i32 %0, %2, 0x19660d\n\ts_add_u32 %0, %0, 0x3c6ef35f\n\ts_lshl_b32 %1, vcc_lo, 3" : "=&s"(tg), "=&s"(c0) : "s"(epoch) : "scc", "vcc");
        c0 -= XL;
        const bool pub = !gh && s == ((0xD840 >> (4 * (b & 3))) & 15);
        st8c(v2u{c0, tg}, rw, (pub && lane < 16) ? WS_BEACON + (b & 7) * RS + lane * 8 : OOB, 0);
        SB();
        float v[9];
#pragma unroll
        for (int k = 0; k < 4; ++k) { v[2 * k] = asf(xa[k] << 16); v[2 * k + 1] = asf(xa[k] & 0xFFFF0000u); }
        v[8] = asf(xb << 16);
        float sm = 0.f;
#pragma unroll
        for (int k = 0; k < 9; ++k) sm += v[k];
        const float mean = sum64(sm) * (1.f / DM), mb = rbf(mean);
        float sq = 0.f;
#pragma unroll
        for (int k = 0; k < 9; ++k) { const float d = v[k] - mean; sq = fmaf(d, d, sq); }
        const float sd = rbf(sqrtf(rbf(rbf(sum64(sq) * (1.f / DM)) + 1e-5f)));
        const float rs = 1.f / sd;
        u32 n[9];
#pragma unroll
        for (int k = 0; k < 9; ++k) { const float a = rbf(v[k] - mb), q0 = a * rs, y = fmaf(fmaf(-q0, sd, a), rs, q0); n[k] = pkbf(y, y); }
        *(LAS v4u*)(lds + L_XN + lane * 32) = v4u{n[0], n[1], n[2], n[3]};
        *(LAS v4u*)(lds + L_XN + lane * 32 + 16) = v4u{n[4], n[5], n[6], n[7]};
        *(LAS u32*)(lds + L_XN + 2048 + lane * 4) = n[8];
        WAITT(c0 + TB);
        u64 b0, b1, b2, b3, b4, b5, b6, b7;
        asm volatile("s_load_dwordx2 %0, %8, %9\n\ts_load_dwordx2 %1, %8, %9+%10\n\ts_load_dwordx2 %2, %8, %9+2*%10\n\ts_load_dwordx2 %3, %8, %9+3*%10\n\t"
                     "s_load_dwordx2 %4, %8, %9+4*%10\n\ts_load_dwordx2 %5, %8, %9+5*%10\n\ts_load_dwordx2 %6, %8, %9+6*%10\n\ts_load_dwordx2 %7, %8, %9+7*%10\n\ts_waitcnt lgkmcnt(0)"
                     : "=&s"(b0), "=&s"(b1), "=&s"(b2), "=&s"(b3), "=&s"(b4), "=&s"(b5), "=&s"(b6), "=&s"(b7) : "s"(wptr), "i"(WS_BEACON), "i"(RS) : "memory");
        const u64 bb[8] = {b0, b1, b2, b3, b4, b5, b6, b7};
        int nb = 0, sm_ = 0;
#pragma unroll
        for (int i = 0; i < 8; ++i) { const u32 d = (u32)bb[i] - c0; const bool ok = (u32)(bb[i] >> 32) == tg && d + 1600u < 3200u; nb += ok; sm_ += ok ? (int)d : 0; }
        const int md = __builtin_amdgcn_readfirstlane(nb > 0 ? (int)((float)sm_ * __builtin_amdgcn_rcpf((float)nb)) + BOFF : 0);
        if (nb >= 4) *(volatile LAS u32*)(lds + L_M + 72) = c0 + min(md, BOFF) - EOFF;
        *(LAS v2u*)(lds + L_M + 64) = v2u{tg, c0 + md};
      }
      const rsrc_t rv = mk_rsrc(vc + hb, WIN * 256);
      MKMASK();
      asm volatile("s_waitcnt lgkmcnt(0)\n\ts_barrier" :: "s"(junk), "s"(junk2) : "memory");
      const u32 tag = GETTAG(), t8 = tag & 255u, t8b = t8 ^ ((tag >> 8) & 254u) ^ 1u;
      const u32 tauv = TV2;
      u32 dl = GETANCHOR() + k0 + 17 * tau - tauv + ERG;
      int wr = L_ORED + w * 1024 + lane * 16, rd = L_ORED + lane * 16, ca = L_CNT + 512 + lane * 4;
      int so = WS_OBOX + s * RS + g * 1024 + ((lane >> 5) * 32 + (lane & 15) * 2 + ((lane >> 4) & 1)) * 16;
      int mo_ = lane < 8 ? WS_ML + s * RS + g * 128 + lane * 16 : OOB, mr = L_M + (lane & 1) * 16;
      asm volatile("" : "+v"(wr), "+v"(rd), "+v"(ca), "+v"(so), "+v"(mo_), "+v"(mr));
      WAITFLAG(4 + w);
      v4u V[16];
      sfor<0, 16>([&](auto T) { constexpr int t = T.value; DPACE(t, WV, tauv); V[t] = ld16(rv, vo, KOFF(t)); });
      SETFLAG(8 + w);
      __builtin_amdgcn_s_setprio(3);
      WAITFLAG(12 + w);
      const v2u pp = *(LAS v2u*)(lds + L_P + w * 512 + lane * 8);
      const u32 pm[2] = {dppu<HMIR>(pp.x), dppu<HMIR>(pp.y)};
      const f4 mm = *(LAS f4*)(lds + mr);
      float mx = fmaxf(fmaxf(mm.x, mm.y), fmaxf(mm.z, mm.w));
      asm volatile("" : "+v"(mx));
      f4 acc = {0.f, 0.f, 0.f, 0.f};
      sfor<0, 16>([&](auto T) { constexpr int t = T.value; acc = mfma(onehot<(t & 7)>(pp[t >> 3], pm[t >> 3], mask), V[t], acc); });
      *(LAS f4*)(lds + wr) = acc;
      FENCE();
      const u32 old = __hip_atomic_fetch_add((LAS u32*)(lds + ca), 1u, __ATOMIC_RELAXED, __HIP_MEMORY_SCOPE_WORKGROUP);
      FENCE();
      const f4 ll = *(LAS f4*)(lds + mr + 32);
      FENCE();
      const f4 a0 = *(LAS f4*)(lds + rd), a1 = *(LAS f4*)(lds + rd + 1024), a2 = *(LAS f4*)(lds + rd + 2048), a3 = *(LAS f4*)(lds + rd + 3072);
      SB();
      u32 sel = 0x07060500u; v4u mv = {asu(mx), tag, 0u, tag};
      so = old == 3 ? so : OOB; mo_ = old == 3 ? mo_ : OOB;
      asm volatile("" : "+v"(so), "+v"(mo_), "+v"(sel), "+v"(mv));
      SB();
      mv.z = asu(((ll.x + ll.y) + ll.z) + ll.w);
      st16c(mv, rw, mo_, 0);
      SB();
      st16c(tagged(((a0 + a1) + a2) + a3, t8, t8b, sel), rw, so, 0);
    }
  }
}

extern "C" void kernel_launch(void* const* d_in, const int* in_sizes, int n_in, void* d_out, int out_size, void* d_ws, size_t ws_size, hipStream_t stream) {
  const char *wq = (const char*)d_in[3], *wo = (const char*)d_in[4], *w1 = (const char*)d_in[6], *w3 = (const char*)d_in[7];
  Args a;
  for (int i = 0; i < 32; ++i) {
    const int s = i >> 1, gh = i & 1;
    Slot& t = a.slot[i];
    if (s >= 13) {
      const u32 off = gh * 8 * 256 * 1152 + (s - 13) * 384;
      t.base = wo + off; t.nrec = 4096 * 1152 - off; t.gmulA = 512 * 1152; t.gmulB = 256 * 1152; t.wmul = 64 * 1152; t.stepA = 4608 - 256; t.stepB = 128;
    } else {
      const u32 stride = s < 4 ? 8192 : 9216, m3 = s == 12 ? gh : (s >= 8);
      const u32 off = s == 12 ? 32 * 256 : (16 * gh + ((s >> 1) & 1)) * 256 + (s & 1) * RK * stride;
      t.base = (s < 4 ? wq : m3 ? w3 : w1) + off; t.nrec = DM * stride - off;
      t.gmulA = s == 12 ? 256 : 1024; t.gmulB = s == 12 ? RK * stride : 512; t.wmul = RW * stride; t.stepA = t.stepB = RT * stride;
    }
  }
  a.x = (const char*)d_in[0]; a.kc = (const char*)d_in[1]; a.vc = (const char*)d_in[2]; a.w2 = (const char*)d_in[8]; a.out = (char*)d_out; a.ws = (char*)d_ws;
  fused_decode<<<256, 768, LDS_BYTES, stream>>>(a);
}
```
